```python
import torch

def _softmax(x, axis=-1):
    return torch.softmax(x, dim=axis)
def _gelu(x):
    return torch.nn.functional.gelu(x, approximate="tanh")
def _silu(x):
    return torch.nn.functional.silu(x)
def _sigmoid(x):
    return torch.sigmoid(x)
def _layernorm(x, axis=-1, eps=1e-5):
    m = x.mean(dim=axis, keepdim=True)
    v = x.var(dim=axis, keepdim=True, unbiased=False)
    return (x - m) / torch.sqrt(v + eps)
def _scatter_add(tgt, idx, src, axis=0):
    return tgt.clone().index_add_(axis, idx, src)
def _segment_sum(src, seg, n):
    out = torch.zeros((n,) + src.shape[1:], dtype=src.dtype)
    return out.index_add_(0, seg, src)
def _pad(x, pw):
    flat = tuple(v for pair in reversed(pw) for v in pair)
    return torch.nn.functional.pad(x, flat)

kernel_name = 'standalone__attention_interact_autoint__recsys'

def setup_inputs():

    _w = (lambda a: (lambda t: t.to(torch.bfloat16) if t.is_floating_point() else t)(torch.as_tensor(a)))
    B, F = 1024, 100
    D = max(2, (64 // 2) * 2)
    H = 2
    Dh = D // H
    return {
        "e": _w((lambda *s: torch.randn(*s, dtype=torch.bfloat16) * 0.05)(B, F, D).to(float)),
        "wq": _w((lambda *s: torch.randn(*s, dtype=torch.bfloat16) * 3.2)(H, D, Dh).to(float)),
        "wk": _w((lambda *s: torch.randn(*s, dtype=torch.bfloat16) * 3.2)(H, D, Dh).to(float)),
        "wv": _w((lambda *s: torch.randn(*s, dtype=torch.bfloat16) * 0.05)(H, D, Dh).to(float)),
        "wres": _w((lambda *s: torch.randn(*s, dtype=torch.bfloat16) * 0.05)(D, D).to(float)),
    }

def reference(e, wq, wk, wv, wres):
    H, D, Dh = wq.shape[0], wq.shape[1], wq.shape[2]
    B, F = e.shape[0], e.shape[1]
    q = torch.einsum('bfd,hdk->bhfk', e, wq)
    k = torch.einsum('bfd,hdk->bhfk', e, wk)
    v = torch.einsum('bfd,hdk->bhfk', e, wv)
    scale = float(Dh) ** -0.5
    scores = torch.einsum('bhfk,bhgk->bhfg', q, k) * scale
    attn = _softmax(scores, axis=-1)
    attn = (lambda x, dt: x.to(dt))(attn, v.dtype)
    ctx = torch.einsum('bhfg,bhgk->bhfk', attn, v)
    ctx = (lambda x, *d: x.permute(*d))(ctx, 0, 2, 1, 3).reshape(B, F, H * Dh)
    res = torch.einsum('bfd,de->bfe', e, wres)
    out = ctx + res
    mask = (lambda x, dt: x.to(dt))(out > 0, out.dtype)
    return out * mask
```

```cpp
#include <hip/hip_runtime.h>
#include <hip/hip_bf16.h>
#include <stdint.h>

typedef __bf16 bf16x8 __attribute__((ext_vector_type(8)));
typedef __bf16 bf16x2 __attribute__((ext_vector_type(2)));
typedef short s16x4 __attribute__((ext_vector_type(4)));
typedef short s16x2 __attribute__((ext_vector_type(2)));
typedef float f32x16 __attribute__((ext_vector_type(16)));
typedef float f32x4 __attribute__((ext_vector_type(4)));
typedef float f32x2 __attribute__((ext_vector_type(2)));
typedef unsigned u32x4 __attribute__((ext_vector_type(4)));
typedef unsigned u32x2 __attribute__((ext_vector_type(2)));
typedef __attribute__((address_space(3))) const char* lds_cptr;
typedef __attribute__((address_space(3))) char* lds_ptr;

#define DI __device__ __forceinline__
#define MFMA32(a, b, c) __builtin_amdgcn_mfma_f32_32x32x16_bf16((a), (b), (c), 0, 0, 0)
#define MFMA16(a, b, c) __builtin_amdgcn_mfma_f32_16x16x32_bf16((a), (b), (c), 0, 0, 0)

constexpr int NF = 100, ND = 64;
constexpr int W_BYTES = 32768, E_BYTES = 104 * 128, LDS_BYTES = W_BYTES + 8 * E_BYTES;
constexpr float CSCALE = 0.17677669529663687f * 1.4426950408889634f;

DI unsigned pk2(float lo, float hi) {
  f32x2 v = {lo, hi};
  return __builtin_bit_cast(unsigned, __builtin_convertvector(v, bf16x2));
}
DI bf16x8 pack8(const f32x16& x, int s) {
  u32x4 p = {pk2(x[8 * s], x[8 * s + 1]), pk2(x[8 * s + 2], x[8 * s + 3]), pk2(x[8 * s + 4], x[8 * s + 5]), pk2(x[8 * s + 6], x[8 * s + 7])};
  return __builtin_bit_cast(bf16x8, p);
}
DI void glds16(const char* sbase, unsigned voff, unsigned lds_base) {
  asm volatile("s_mov_b32 m0, %2\n\ts_nop 0\n\tglobal_load_lds_dwordx4 %0, %1" ::"v"(voff), "s"(sbase), "s"(lds_base) : "memory");
}
DI s16x4 vtr(lds_cptr p) { return __builtin_amdgcn_ds_read_tr16_b64_v4i16((__attribute__((address_space(3))) s16x4*)p); }
DI bf16x8 wfrag(lds_cptr p, int off) {
  s16x4 lo = vtr(p + off), hi = vtr(p + off + 256);
  return __builtin_bit_cast(bf16x8, __builtin_shufflevector(lo, hi, 0, 1, 2, 3, 4, 5, 6, 7));
}
DI bf16x8 lds16(lds_cptr p) { return *(const __attribute__((address_space(3))) bf16x8*)p; }
#define MX3(a, b, c) __builtin_fmaxf(__builtin_fmaxf((a), (b)), (c))

__global__ __launch_bounds__(512, 2) void autoint_kernel(const char* __restrict__ e, const char* __restrict__ wq, const char* __restrict__ wk,
                                                         const char* __restrict__ wv, const char* __restrict__ wr, char* __restrict__ out) {
  extern __shared__ __attribute__((aligned(16))) char lds[];
  const int lane = threadIdx.x & 63;
  const int w = __builtin_amdgcn_readfirstlane(threadIdx.x >> 6);
  const int hd = w & 1;
  const int b = blockIdx.x * 4 + (w >> 1);
  const unsigned lds0 = (unsigned)(uintptr_t)lds;
  const int r = lane & 31, h = lane >> 5;

  {
    const unsigned wo = w * 1024 + lane * 16;
    glds16(wq, wo, lds0 + w * 1024);
    glds16(wk, wo, lds0 + 8192 + w * 1024);
    glds16(wv, wo, lds0 + 16384 + w * 1024);
    const unsigned ci = w * 64 + lane;
    glds16(wr, ((ci >> 2) & 63) * 128 + (ci >> 8) * 64 + (ci & 3) * 16, lds0 + 24576 + w * 1024);
  }
  const unsigned ebase = lds0 + W_BYTES + w * E_BYTES;
  {
    const char* eb = e + (size_t)b * (NF * ND * 2);
    const unsigned vE = (lane >> 3) * 128 + (((lane & 7) ^ (lane >> 4)) * 16);
    const unsigned vO = vE ^ 64;
#pragma unroll
    for (int n = 0; n < 12; ++n) glds16(eb + n * 1024, (n & 1) ? vO : vE, ebase + n * 1024);
    if (lane < 32) glds16(eb + 12 * 1024, vE, ebase + 12 * 1024);
  }
  asm volatile("s_waitcnt vmcnt(0)\n\ts_barrier" ::: "memory");

  const int t16 = lane & 15, beta = (lane >> 4) & 1, p4 = t16 & 3, q4 = t16 >> 2;
  const lds_cptr wp = (lds_cptr)lds + hd * 4096 + (8 * h + q4) * 64 + (16 * (p4 & 1) + 8 * beta + 4 * (p4 >> 1)) * 2;
  const lds_cptr ep = (lds_cptr)lds + W_BYTES + w * E_BYTES;
  int eoff[4], eoff3[4];
  const int rho = 2 * ((r >> 2) & 1) + (r & 1);
#pragma unroll
  for (int s = 0; s < 4; ++s) {
    eoff[s] = r * 128 + 16 * ((2 * s + h) ^ ((r >> 1) & 7));
    eoff3[s] = (96 + rho) * 128 + 16 * ((2 * s + h) ^ (rho >> 1));
  }
  const f32x16 z16 = {};

  bf16x8 kf[4][2], vf[4][2];
  {
    bf16x8 wkf[4], wvf[4];
#pragma unroll
    for (int s = 0; s < 4; ++s) { wkf[s] = wfrag(wp, 8192 + s * 1024); wvf[s] = wfrag(wp, 16384 + s * 1024); }
#pragma unroll
    for (int T = 0; T < 4; ++T) {
      bf16x8 ef[4];
#pragma unroll
      for (int s = 0; s < 4; ++s) ef[s] = lds16(ep + (T < 3 ? eoff[s] + T * 4096 : eoff3[s]));
      f32x16 ka = z16, va = z16;
#pragma unroll
      for (int s = 0; s < 4; ++s) { ka = MFMA32(wkf[s], ef[s], ka); va = MFMA32(ef[s], wvf[s], va); }
      kf[T][0] = pack8(ka, 0); kf[T][1] = pack8(ka, 1);
      vf[T][0] = pack8(va, 0); if (T < 3) vf[T][1] = pack8(va, 1);
    }
  }
  bf16x8 wqf[4], wrf[4];
#pragma unroll
  for (int s = 0; s < 4; ++s) { wqf[s] = wfrag(wp, s * 1024); wrf[s] = wfrag(wp, 24576 + s * 1024); }
  bf16x8 ones;
  { const unsigned o = (((t16 >> 2) & 1) == beta) ? 0x3f803f80u : 0u; u32x4 o4 = {o, o, o, o}; ones = __builtin_bit_cast(bf16x8, o4); }

  char* ob = out + (size_t)b * (NF * ND * 2) + hd * 64 + h * 32;
#pragma unroll 1
  for (int Tq = 0; Tq < 4; ++Tq) {
    bf16x8 ef[4];
#pragma unroll
    for (int s = 0; s < 4; ++s) ef[s] = lds16(ep + (Tq < 3 ? eoff[s] + Tq * 4096 : eoff3[s]));
    f32x16 qa = z16, ra = z16;
#pragma unroll
    for (int s = 0; s < 4; ++s) { qa = MFMA32(wqf[s], ef[s], qa); ra = MFMA32(wrf[s], ef[s], ra); }
    const bf16x8 q0 = pack8(qa, 0), q1 = pack8(qa, 1);
    f32x16 sc[4];
#pragma unroll
    for (int T = 0; T < 4; ++T) { sc[T] = MFMA32(kf[T][0], q0, z16); sc[T] = MFMA32(kf[T][1], q1, sc[T]); }
    float m0 = __builtin_fmaxf(sc[3][0], sc[3][1]), m1 = __builtin_fmaxf(sc[0][0], sc[0][1]);
#pragma unroll
    for (int i = 2; i < 16; i += 2) m1 = MX3(m1, sc[0][i], sc[0][i + 1]);
#pragma unroll
    for (int i = 0; i < 16; i += 2) { m0 = MX3(m0, sc[1][i], sc[1][i + 1]); m1 = MX3(m1, sc[2][i], sc[2][i + 1]); }
    float m = __builtin_fmaxf(m0, m1);
    { auto rr = __builtin_amdgcn_permlane32_swap(__float_as_uint(m), __float_as_uint(m), false, false);
      m = __builtin_fmaxf(__uint_as_float(rr[0]), __uint_as_float(rr[1])); }
    const float nm = -m * CSCALE;
    float cs = CSCALE; asm volatile("" : "+v"(cs));
    f32x16 oa = z16; f32x4 la = {};
#pragma unroll
    for (int T = 0; T < 3; ++T) {
#pragma unroll
      for (int i = 0; i < 16; ++i) sc[T][i] = __builtin_amdgcn_exp2f(__builtin_fmaf(sc[T][i], cs, nm));
      const bf16x8 p0 = pack8(sc[T], 0), p1 = pack8(sc[T], 1);
      oa = MFMA32(vf[T][0], p0, oa); la = MFMA16(ones, p0, la);
      oa = MFMA32(vf[T][1], p1, oa); la = MFMA16(ones, p1, la);
    }
    {
      const float e0 = __builtin_amdgcn_exp2f(__builtin_fmaf(sc[3][0], cs, nm)), e1 = __builtin_amdgcn_exp2f(__builtin_fmaf(sc[3][1], cs, nm));
      u32x4 p = {pk2(e0, e1), 0u, 0u, 0u};
      const bf16x8 p0 = __builtin_bit_cast(bf16x8, p);
      oa = MFMA32(vf[3][0], p0, oa); la = MFMA16(ones, p0, la);
    }
    const float rl = __builtin_amdgcn_rcpf(la[0]);
    unsigned o[8];
#pragma unroll
    for (int i = 0; i < 8; ++i) {
      const unsigned u = pk2(__builtin_fmaf(oa[2 * i], rl, ra[2 * i]), __builtin_fmaf(oa[2 * i + 1], rl, ra[2 * i + 1]));
      const s16x2 zz = {0, 0};
      o[i] = __builtin_bit_cast(unsigned, __builtin_elementwise_max(__builtin_bit_cast(s16x2, u), zz));
    }
    const int row = Tq < 3 ? Tq * 32 + r : 96 + rho;
    if (Tq < 3 || (r & ~5) == 0) {
      u32x4 o0 = {o[0], o[1], o[2], o[3]}, o1 = {o[4], o[5], o[6], o[7]};
      __builtin_nontemporal_store(o0, (u32x4*)(ob + row * 128));
      __builtin_nontemporal_store(o1, (u32x4*)(ob + row * 128 + 16));
    }
  }
}

extern "C" void kernel_launch(void* const* d_in, const int* in_sizes, int n_in, void* d_out, int out_size, void* d_ws, size_t ws_size,
                              hipStream_t stream) {
  (void)in_sizes; (void)n_in; (void)out_size; (void)d_ws; (void)ws_size;
  hipFuncSetAttribute((const void*)autoint_kernel, hipFuncAttributeMaxDynamicSharedMemorySize, LDS_BYTES);
  autoint_kernel<<<256, 512, LDS_BYTES, stream>>>((const char*)d_in[0], (const char*)d_in[1], (const char*)d_in[2], (const char*)d_in[3],
                                                   (const char*)d_in[4], (char*)d_out);
}
```

```cpp
#include <hip/hip_runtime.h>
#include <hip/hip_bf16.h>
#include <stdint.h>

typedef __bf16 bf16x8 __attribute__((ext_vector_type(8)));
typedef __bf16 bf16x2 __attribute__((ext_vector_type(2)));
typedef short s16x4 __attribute__((ext_vector_type(4)));
typedef short s16x2 __attribute__((ext_vector_type(2)));
typedef float f32x16 __attribute__((ext_vector_type(16)));
typedef float f32x4 __attribute__((ext_vector_type(4)));
typedef float f32x2 __attribute__((ext_vector_type(2)));
typedef unsigned u32x4 __attribute__((ext_vector_type(4)));
typedef unsigned u32x2 __attribute__((ext_vector_type(2)));
typedef __attribute__((address_space(3))) const char* lds_cptr;
typedef __attribute__((address_space(3))) char* lds_ptr;

#define DI __device__ __forceinline__
#define MFMA32(a, b, c) __builtin_amdgcn_mfma_f32_32x32x16_bf16((a), (b), (c), 0, 0, 0)
#define MFMA16(a, b, c) __builtin_amdgcn_mfma_f32_16x16x32_bf16((a), (b), (c), 0, 0, 0)

constexpr int NF = 100, ND = 64;
constexpr int W_BYTES = 32768, E_BYTES = 104 * 128, LDS_BYTES = W_BYTES + 8 * E_BYTES;
constexpr float CSCALE = 0.17677669529663687f * 1.4426950408889634f;

DI unsigned pk2(float lo, float hi) {
  f32x2 v = {lo, hi};
  return __builtin_bit_cast(unsigned, __builtin_convertvector(v, bf16x2));
}
DI bf16x8 pack8(const f32x16& x, int s) {
  u32x4 p = {pk2(x[8 * s], x[8 * s + 1]), pk2(x[8 * s + 2], x[8 * s + 3]), pk2(x[8 * s + 4], x[8 * s + 5]), pk2(x[8 * s + 6], x[8 * s + 7])};
  return __builtin_bit_cast(bf16x8, p);
}
DI void glds16(const char* sbase, unsigned voff, unsigned lds_base) {
  asm volatile("s_mov_b32 m0, %2\n\ts_nop 0\n\tglobal_load_lds_dwordx4 %0, %1" ::"v"(voff), "s"(sbase), "s"(lds_base) : "memory");
}
DI void glds16nt(const char* sbase, unsigned voff, unsigned lds_base) {
  asm volatile("s_mov_b32 m0, %2\n\ts_nop 0\n\tglobal_load_lds_dwordx4 %0, %1 nt" ::"v"(voff), "s"(sbase), "s"(lds_base) : "memory");
}
DI s16x4 vtr(lds_cptr p) { return __builtin_amdgcn_ds_read_tr16_b64_v4i16((__attribute__((address_space(3))) s16x4*)p); }
DI bf16x8 wfrag(lds_cptr p, int off) {
  s16x4 lo = vtr(p + off), hi = vtr(p + off + 256);
  return __builtin_bit_cast(bf16x8, __builtin_shufflevector(lo, hi, 0, 1, 2, 3, 4, 5, 6, 7));
}
DI bf16x8 lds16(lds_cptr p) { return *(const __attribute__((address_space(3))) bf16x8*)p; }
#define MX3(a, b, c) __builtin_fmaxf(__builtin_fmaxf((a), (b)), (c))

__global__ __launch_bounds__(512, 2) void autoint_kernel(const char* __restrict__ e, const char* __restrict__ wq, const char* __restrict__ wk,
                                                         const char* __restrict__ wv, const char* __restrict__ wr, char* __restrict__ out) {
  extern __shared__ __attribute__((aligned(16))) char lds[];
  const int lane = threadIdx.x & 63;
  const int w = __builtin_amdgcn_readfirstlane(threadIdx.x >> 6);
  const int hd = w & 1;
  const int b = blockIdx.x * 4 + (w >> 1);
  const unsigned lds0 = (unsigned)(uintptr_t)lds;
  const int r = lane & 31, h = lane >> 5;

  {
    const unsigned wo = w * 1024 + lane * 16;
    glds16(wq, wo, lds0 + w * 1024);
    glds16(wk, wo, lds0 + 8192 + w * 1024);
    glds16(wv, wo, lds0 + 16384 + w * 1024);
    const unsigned ci = w * 64 + lane;
    glds16(wr, ((ci >> 2) & 63) * 128 + (ci >> 8) * 64 + (ci & 3) * 16, lds0 + 24576 + w * 1024);
  }
  const unsigned ebase = lds0 + W_BYTES + w * E_BYTES;
  {
    const char* eb = e + (size_t)b * (NF * ND * 2);
    const unsigned vE = (lane >> 3) * 128 + (((lane & 7) ^ (lane >> 4)) * 16);
    const unsigned vO = vE ^ 64;
#pragma unroll
    for (int n = 0; n < 12; ++n) glds16nt(eb + n * 1024, (n & 1) ? vO : vE, ebase + n * 1024);
    if (lane < 32) glds16nt(eb + 12 * 1024, vE, ebase + 12 * 1024);
  }
  asm volatile("s_waitcnt vmcnt(0)\n\ts_barrier" ::: "memory");

  const int t16 = lane & 15, beta = (lane >> 4) & 1, p4 = t16 & 3, q4 = t16 >> 2;
  const lds_cptr wp = (lds_cptr)lds + hd * 4096 + (8 * h + q4) * 64 + (16 * (p4 & 1) + 8 * beta + 4 * (p4 >> 1)) * 2;
  const lds_cptr ep = (lds_cptr)lds + W_BYTES + w * E_BYTES;
  int eoff[4], eoff3[4];
  const int rho = 2 * ((r >> 2) & 1) + (r & 1);
#pragma unroll
  for (int s = 0; s < 4; ++s) {
    eoff[s] = r * 128 + 16 * ((2 * s + h) ^ ((r >> 1) & 7));
    eoff3[s] = (96 + rho) * 128 + 16 * ((2 * s + h) ^ (rho >> 1));
  }
  const f32x16 z16 = {};

  bf16x8 kf[4][2], vf[4][2];
  {
    bf16x8 wkf[4], wvf[4];
#pragma unroll
    for (int s = 0; s < 4; ++s) { wkf[s] = wfrag(wp, 8192 + s * 1024); wvf[s] = wfrag(wp, 16384 + s * 1024); }
#pragma unroll
    for (int T = 0; T < 4; ++T) {
      bf16x8 ef[4];
#pragma unroll
      for (int s = 0; s < 4; ++s) ef[s] = lds16(ep + (T < 3 ? eoff[s] + T * 4096 : eoff3[s]));
      f32x16 ka = z16, va = z16;
#pragma unroll
      for (int s = 0; s < 4; ++s) { ka = MFMA32(wkf[s], ef[s], ka); va = MFMA32(ef[s], wvf[s], va); }
      kf[T][0] = pack8(ka, 0); kf[T][1] = pack8(ka, 1);
      vf[T][0] = pack8(va, 0); if (T < 3) vf[T][1] = pack8(va, 1);
    }
  }
  bf16x8 wqf[4], wrf[4];
#pragma unroll
  for (int s = 0; s < 4; ++s) { wqf[s] = wfrag(wp, s * 1024); wrf[s] = wfrag(wp, 24576 + s * 1024); }
  bf16x8 ones;
  { const unsigned o = (((t16 >> 2) & 1) == beta) ? 0x3f803f80u : 0u; u32x4 o4 = {o, o, o, o}; ones = __builtin_bit_cast(bf16x8, o4); }

  char* ob = out + (size_t)b * (NF * ND * 2) + hd * 64 + h * 32;
#pragma unroll 1
  for (int Tq = 0; Tq < 4; ++Tq) {
    bf16x8 ef[4];
#pragma unroll
    for (int s = 0; s < 4; ++s) ef[s] = lds16(ep + (Tq < 3 ? eoff[s] + Tq * 4096 : eoff3[s]));
    f32x16 qa = z16, ra = z16;
#pragma unroll
    for (int s = 0; s < 4; ++s) { qa = MFMA32(wqf[s], ef[s], qa); ra = MFMA32(wrf[s], ef[s], ra); }
    const bf16x8 q0 = pack8(qa, 0), q1 = pack8(qa, 1);
    f32x16 sc[4];
#pragma unroll
    for (int T = 0; T < 4; ++T) { sc[T] = MFMA32(kf[T][0], q0, z16); sc[T] = MFMA32(kf[T][1], q1, sc[T]); }
    float m0 = __builtin_fmaxf(sc[3][0], sc[3][1]), m1 = __builtin_fmaxf(sc[0][0], sc[0][1]);
#pragma unroll
    for (int i = 2; i < 16; i += 2) m1 = MX3(m1, sc[0][i], sc[0][i + 1]);
#pragma unroll
    for (int i = 0; i < 16; i += 2) { m0 = MX3(m0, sc[1][i], sc[1][i + 1]); m1 = MX3(m1, sc[2][i], sc[2][i + 1]); }
    float m = __builtin_fmaxf(m0, m1);
    { auto rr = __builtin_amdgcn_permlane32_swap(__float_as_uint(m), __float_as_uint(m), false, false);
      m = __builtin_fmaxf(__uint_as_float(rr[0]), __uint_as_float(rr[1])); }
    const float nm = -m * CSCALE;
    float cs = CSCALE; asm volatile("" : "+v"(cs));
    f32x16 oa = z16; f32x4 la = {};
#pragma unroll
    for (int T = 0; T < 3; ++T) {
#pragma unroll
      for (int i = 0; i < 16; ++i) sc[T][i] = __builtin_amdgcn_exp2f(__builtin_fmaf(sc[T][i], cs, nm));
      const bf16x8 p0 = pack8(sc[T], 0), p1 = pack8(sc[T], 1);
      oa = MFMA32(vf[T][0], p0, oa); la = MFMA16(ones, p0, la);
      oa = MFMA32(vf[T][1], p1, oa); la = MFMA16(ones, p1, la);
    }
    {
      const float e0 = __builtin_amdgcn_exp2f(__builtin_fmaf(sc[3][0], cs, nm)), e1 = __builtin_amdgcn_exp2f(__builtin_fmaf(sc[3][1], cs, nm));
      u32x4 p = {pk2(e0, e1), 0u, 0u, 0u};
      const bf16x8 p0 = __builtin_bit_cast(bf16x8, p);
      oa = MFMA32(vf[3][0], p0, oa); la = MFMA16(ones, p0, la);
    }
    const float rl = __builtin_amdgcn_rcpf(la[0]);
    unsigned o[8];
#pragma unroll
    for (int i = 0; i < 8; ++i) {
      const unsigned u = pk2(__builtin_fmaf(oa[2 * i], rl, ra[2 * i]), __builtin_fmaf(oa[2 * i + 1], rl, ra[2 * i + 1]));
      const s16x2 zz = {0, 0};
      o[i] = __builtin_bit_cast(unsigned, __builtin_elementwise_max(__builtin_bit_cast(s16x2, u), zz));
    }
    const int row = Tq < 3 ? Tq * 32 + r : 96 + rho;
    if (Tq < 3 || (r & ~5) == 0) {
      u32x4 o0 = {o[0], o[1], o[2], o[3]}, o1 = {o[4], o[5], o[6], o[7]};
      __builtin_nontemporal_store(o0, (u32x4*)(ob + row * 128));
      __builtin_nontemporal_store(o1, (u32x4*)(ob + row * 128 + 16));
    }
  }
}

extern "C" void kernel_launch(void* const* d_in, const int* in_sizes, int n_in, void* d_out, int out_size, void* d_ws, size_t ws_size,
                              hipStream_t stream) {
  (void)in_sizes; (void)n_in; (void)out_size; (void)d_ws; (void)ws_size;
  hipFuncSetAttribute((const void*)autoint_kernel, hipFuncAttributeMaxDynamicSharedMemorySize, LDS_BYTES);
  autoint_kernel<<<256, 512, LDS_BYTES, stream>>>((const char*)d_in[0], (const char*)d_in[1], (const char*)d_in[2], (const char*)d_in[3],
                                                   (const char*)d_in[4], (char*)d_out);
}
```

```cpp
#include <hip/hip_runtime.h>
#include <hip/hip_bf16.h>
#include <stdint.h>

typedef __bf16 bf16x8 __attribute__((ext_vector_type(8)));
typedef __bf16 bf16x2 __attribute__((ext_vector_type(2)));
typedef short s16x4 __attribute__((ext_vector_type(4)));
typedef short s16x2 __attribute__((ext_vector_type(2)));
typedef float f32x16 __attribute__((ext_vector_type(16)));
typedef float f32x4 __attribute__((ext_vector_type(4)));
typedef float f32x2 __attribute__((ext_vector_type(2)));
typedef unsigned u32x4 __attribute__((ext_vector_type(4)));
typedef unsigned u32x2 __attribute__((ext_vector_type(2)));
typedef __attribute__((address_space(3))) const char* lds_cptr;
typedef __attribute__((address_space(3))) char* lds_ptr;

#define DI __device__ __forceinline__
#define MFMA32(a, b, c) __builtin_amdgcn_mfma_f32_32x32x16_bf16((a), (b), (c), 0, 0, 0)
#define MFMA16(a, b, c) __builtin_amdgcn_mfma_f32_16x16x32_bf16((a), (b), (c), 0, 0, 0)

constexpr int NF = 100, ND = 64;
constexpr int W_BYTES = 32768, E_BYTES = 104 * 128, LDS_BYTES = W_BYTES + 8 * E_BYTES;
constexpr float CSCALE = 0.17677669529663687f * 1.4426950408889634f;

DI unsigned pk2(float lo, float hi) {
  f32x2 v = {lo, hi};
  return __builtin_bit_cast(unsigned, __builtin_convertvector(v, bf16x2));
}
DI bf16x8 pack8(const f32x16& x, int s) {
  u32x4 p = {pk2(x[8 * s], x[8 * s + 1]), pk2(x[8 * s + 2], x[8 * s + 3]), pk2(x[8 * s + 4], x[8 * s + 5]), pk2(x[8 * s + 6], x[8 * s + 7])};
  return __builtin_bit_cast(bf16x8, p);
}
DI void glds16(const char* sbase, unsigned voff, unsigned lds_base) {
  asm volatile("s_mov_b32 m0, %2\n\ts_nop 0\n\tglobal_load_lds_dwordx4 %0, %1" ::"v"(voff), "s"(sbase), "s"(lds_base) : "memory");
}
DI s16x4 vtr(lds_cptr p) { return __builtin_amdgcn_ds_read_tr16_b64_v4i16((__attribute__((address_space(3))) s16x4*)p); }
DI bf16x8 wfrag(lds_cptr p, int off) {
  s16x4 lo = vtr(p + off), hi = vtr(p + off + 256);
  return __builtin_bit_cast(bf16x8, __builtin_shufflevector(lo, hi, 0, 1, 2, 3, 4, 5, 6, 7));
}
DI bf16x8 lds16(lds_cptr p) { return *(const __attribute__((address_space(3))) bf16x8*)p; }
#define MX3(a, b, c) __builtin_fmaxf(__builtin_fmaxf((a), (b)), (c))

__global__ __launch_bounds__(512, 2) void autoint_kernel(const char* __restrict__ e, const char* __restrict__ wq, const char* __restrict__ wk,
                                                         const char* __restrict__ wv, const char* __restrict__ wr, char* __restrict__ out) {
  const unsigned long long rt0 = __builtin_amdgcn_s_memrealtime();
  extern __shared__ __attribute__((aligned(16))) char lds[];
  const int lane = threadIdx.x & 63;
  const int w = __builtin_amdgcn_readfirstlane(threadIdx.x >> 6);
  const int hd = w & 1;
  const int b = blockIdx.x * 4 + (w >> 1);
  const unsigned lds0 = (unsigned)(uintptr_t)lds;
  const int r = lane & 31, h = lane >> 5;

  {
    const unsigned wo = w * 1024 + lane * 16;
    glds16(wq, wo, lds0 + w * 1024);
    glds16(wk, wo, lds0 + 8192 + w * 1024);
    glds16(wv, wo, lds0 + 16384 + w * 1024);
    const unsigned ci = w * 64 + lane;
    glds16(wr, ((ci >> 2) & 63) * 128 + (ci >> 8) * 64 + (ci & 3) * 16, lds0 + 24576 + w * 1024);
  }
  const unsigned ebase = lds0 + W_BYTES + w * E_BYTES;
  {
    const char* eb = e + (size_t)b * (NF * ND * 2);
    const unsigned vE = (lane >> 3) * 128 + (((lane & 7) ^ (lane >> 4)) * 16);
    const unsigned vO = vE ^ 64;
#pragma unroll
    for (int n = 0; n < 12; ++n) glds16(eb + n * 1024, (n & 1) ? vO : vE, ebase + n * 1024);
    if (lane < 32) glds16(eb + 12 * 1024, vE, ebase + 12 * 1024);
  }
  asm volatile("s_waitcnt vmcnt(0)\n\ts_barrier" ::: "memory");
  const unsigned long long rt1 = __builtin_amdgcn_s_memrealtime();
  const int t16 = lane & 15, beta = (lane >> 4) & 1, p4 = t16 & 3, q4 = t16 >> 2;
  const lds_cptr wp = (lds_cptr)lds + hd * 4096 + (8 * h + q4) * 64 + (16 * (p4 & 1) + 8 * beta + 4 * (p4 >> 1)) * 2;
  const lds_cptr ep = (lds_cptr)lds + W_BYTES + w * E_BYTES;
  int eoff[4], eoff3[4];
  const int rho = 2 * ((r >> 2) & 1) + (r & 1);
#pragma unroll
  for (int s = 0; s < 4; ++s) {
    eoff[s] = r * 128 + 16 * ((2 * s + h) ^ ((r >> 1) & 7));
    eoff3[s] = (96 + rho) * 128 + 16 * ((2 * s + h) ^ (rho >> 1));
  }
  const f32x16 z16 = {};

  bf16x8 kf[4][2], vf[4][2];
  {
    bf16x8 wkf[4], wvf[4];
#pragma unroll
    for (int s = 0; s < 4; ++s) { wkf[s] = wfrag(wp, 8192 + s * 1024); wvf[s] = wfrag(wp, 16384 + s * 1024); }
#pragma unroll
    for (int T = 0; T < 4; ++T) {
      bf16x8 ef[4];
#pragma unroll
      for (int s = 0; s < 4; ++s) ef[s] = lds16(ep + (T < 3 ? eoff[s] + T * 4096 : eoff3[s]));
      f32x16 ka = z16, va = z16;
#pragma unroll
      for (int s = 0; s < 4; ++s) { ka = MFMA32(wkf[s], ef[s], ka); va = MFMA32(ef[s], wvf[s], va); }
      kf[T][0] = pack8(ka, 0); kf[T][1] = pack8(ka, 1);
      vf[T][0] = pack8(va, 0); if (T < 3) vf[T][1] = pack8(va, 1);
    }
  }
  bf16x8 wqf[4], wrf[4];
#pragma unroll
  for (int s = 0; s < 4; ++s) { wqf[s] = wfrag(wp, s * 1024); wrf[s] = wfrag(wp, 24576 + s * 1024); }
  bf16x8 ones;
  { const unsigned o = (((t16 >> 2) & 1) == beta) ? 0x3f803f80u : 0u; u32x4 o4 = {o, o, o, o}; ones = __builtin_bit_cast(bf16x8, o4); }

  char* ob = out + (size_t)b * (NF * ND * 2) + hd * 64 + h * 32;
#pragma unroll 1
  for (int Tq = 0; Tq < 4; ++Tq) {
    bf16x8 ef[4];
#pragma unroll
    for (int s = 0; s < 4; ++s) ef[s] = lds16(ep + (Tq < 3 ? eoff[s] + Tq * 4096 : eoff3[s]));
    f32x16 qa = z16, ra = z16;
#pragma unroll
    for (int s = 0; s < 4; ++s) { qa = MFMA32(wqf[s], ef[s], qa); ra = MFMA32(wrf[s], ef[s], ra); }
    const bf16x8 q0 = pack8(qa, 0), q1 = pack8(qa, 1);
    f32x16 sc[4];
#pragma unroll
    for (int T = 0; T < 4; ++T) { sc[T] = MFMA32(kf[T][0], q0, z16); sc[T] = MFMA32(kf[T][1], q1, sc[T]); }
    float m0 = __builtin_fmaxf(sc[3][0], sc[3][1]), m1 = __builtin_fmaxf(sc[0][0], sc[0][1]);
#pragma unroll
    for (int i = 2; i < 16; i += 2) m1 = MX3(m1, sc[0][i], sc[0][i + 1]);
#pragma unroll
    for (int i = 0; i < 16; i += 2) { m0 = MX3(m0, sc[1][i], sc[1][i + 1]); m1 = MX3(m1, sc[2][i], sc[2][i + 1]); }
    float m = __builtin_fmaxf(m0, m1);
    { auto rr = __builtin_amdgcn_permlane32_swap(__float_as_uint(m), __float_as_uint(m), false, false);
      m = __builtin_fmaxf(__uint_as_float(rr[0]), __uint_as_float(rr[1])); }
    const float nm = -m * CSCALE;
    float cs = CSCALE; asm volatile("" : "+v"(cs));
    f32x16 oa = z16; f32x4 la = {};
#pragma unroll
    for (int T = 0; T < 3; ++T) {
#pragma unroll
      for (int i = 0; i < 16; ++i) sc[T][i] = __builtin_amdgcn_exp2f(__builtin_fmaf(sc[T][i], cs, nm));
      const bf16x8 p0 = pack8(sc[T], 0), p1 = pack8(sc[T], 1);
      oa = MFMA32(vf[T][0], p0, oa); la = MFMA16(ones, p0, la);
      oa = MFMA32(vf[T][1], p1, oa); la = MFMA16(ones, p1, la);
    }
    {
      const float e0 = __builtin_amdgcn_exp2f(__builtin_fmaf(sc[3][0], cs, nm)), e1 = __builtin_amdgcn_exp2f(__builtin_fmaf(sc[3][1], cs, nm));
      u32x4 p = {pk2(e0, e1), 0u, 0u, 0u};
      const bf16x8 p0 = __builtin_bit_cast(bf16x8, p);
      oa = MFMA32(vf[3][0], p0, oa); la = MFMA16(ones, p0, la);
    }
    const float rl = __builtin_amdgcn_rcpf(la[0]);
    unsigned o[8];
#pragma unroll
    for (int i = 0; i < 8; ++i) {
      const unsigned u = pk2(__builtin_fmaf(oa[2 * i], rl, ra[2 * i]), __builtin_fmaf(oa[2 * i + 1], rl, ra[2 * i + 1]));
      const s16x2 zz = {0, 0};
      o[i] = __builtin_bit_cast(unsigned, __builtin_elementwise_max(__builtin_bit_cast(s16x2, u), zz));
    }
    const int row = Tq < 3 ? Tq * 32 + r : 96 + rho;
    if (Tq < 3 || (r & ~5) == 0) {
      u32x4 o0 = {o[0], o[1], o[2], o[3]}, o1 = {o[4], o[5], o[6], o[7]};
      asm volatile("global_store_dwordx4 %0, %1, off nt\n\tglobal_store_dwordx4 %0, %2, off offset:16 nt\n\ts_nop 1" :: "v"(ob + row * 128), "v"(o0), "v"(o1) : "memory");
    }
  }
  { const unsigned long long tgt = rt0 + 4000 + 0 * rt1; while (__builtin_amdgcn_s_memrealtime() < tgt) __builtin_amdgcn_s_sleep(2); }
}

extern "C" void kernel_launch(void* const* d_in, const int* in_sizes, int n_in, void* d_out, int out_size, void* d_ws, size_t ws_size,
                              hipStream_t stream) {
  (void)in_sizes; (void)n_in; (void)out_size; (void)d_ws; (void)ws_size;
  hipFuncSetAttribute((const void*)autoint_kernel, hipFuncAttributeMaxDynamicSharedMemorySize, LDS_BYTES);
  autoint_kernel<<<256, 512, LDS_BYTES, stream>>>((const char*)d_in[0], (const char*)d_in[1], (const char*)d_in[2], (const char*)d_in[3],
                                                   (const char*)d_in[4], (char*)d_out);
}
```

```cpp
#include <hip/hip_runtime.h>
#include <hip/hip_bf16.h>
#include <stdint.h>

typedef __bf16 bf16x8 __attribute__((ext_vector_type(8)));
typedef __bf16 bf16x2 __attribute__((ext_vector_type(2)));
typedef short s16x4 __attribute__((ext_vector_type(4)));
typedef short s16x2 __attribute__((ext_vector_type(2)));
typedef float f32x16 __attribute__((ext_vector_type(16)));
typedef float f32x4 __attribute__((ext_vector_type(4)));
typedef float f32x2 __attribute__((ext_vector_type(2)));
typedef unsigned u32x4 __attribute__((ext_vector_type(4)));
typedef unsigned u32x2 __attribute__((ext_vector_type(2)));
typedef __attribute__((address_space(3))) const char* lds_cptr;
typedef __attribute__((address_space(3))) char* lds_ptr;

#define DI __device__ __forceinline__
#define MFMA32(a, b, c) __builtin_amdgcn_mfma_f32_32x32x16_bf16((a), (b), (c), 0, 0, 0)
#define MFMA16(a, b, c) __builtin_amdgcn_mfma_f32_16x16x32_bf16((a), (b), (c), 0, 0, 0)

constexpr int NF = 100, ND = 64;
constexpr int W_BYTES = 32768, E_BYTES = 104 * 128, LDS_BYTES = W_BYTES + 8 * E_BYTES;
constexpr float CSCALE = 0.17677669529663687f * 1.4426950408889634f;

DI unsigned pk2(float lo, float hi) {
  f32x2 v = {lo, hi};
  return __builtin_bit_cast(unsigned, __builtin_convertvector(v, bf16x2));
}
DI bf16x8 pack8(const f32x16& x, int s) {
  u32x4 p = {pk2(x[8 * s], x[8 * s + 1]), pk2(x[8 * s + 2], x[8 * s + 3]), pk2(x[8 * s + 4], x[8 * s + 5]), pk2(x[8 * s + 6], x[8 * s + 7])};
  return __builtin_bit_cast(bf16x8, p);
}
DI void glds16(const char* sbase, unsigned voff, unsigned lds_base) {
  asm volatile("s_mov_b32 m0, %2\n\ts_nop 0\n\tglobal_load_lds_dwordx4 %0, %1" ::"v"(voff), "s"(sbase), "s"(lds_base) : "memory");
}
DI s16x4 vtr(lds_cptr p) { return __builtin_amdgcn_ds_read_tr16_b64_v4i16((__attribute__((address_space(3))) s16x4*)p); }
DI bf16x8 wfrag(lds_cptr p, int off) {
  s16x4 lo = vtr(p + off), hi = vtr(p + off + 256);
  return __builtin_bit_cast(bf16x8, __builtin_shufflevector(lo, hi, 0, 1, 2, 3, 4, 5, 6, 7));
}
DI bf16x8 lds16(lds_cptr p) { return *(const __attribute__((address_space(3))) bf16x8*)p; }
#define MX3(a, b, c) __builtin_fmaxf(__builtin_fmaxf((a), (b)), (c))

__global__ __launch_bounds__(512, 2) void autoint_kernel(const char* __restrict__ e, const char* __restrict__ wq, const char* __restrict__ wk,
                                                         const char* __restrict__ wv, const char* __restrict__ wr, char* __restrict__ out, unsigned long long* gmin) {
  const unsigned long long rt0 = __builtin_amdgcn_s_memrealtime();
  if ((threadIdx.x & 63) == 0) __hip_atomic_fetch_min(gmin, rt0, __ATOMIC_RELAXED, __HIP_MEMORY_SCOPE_AGENT);
  extern __shared__ __attribute__((aligned(16))) char lds[];
  const int lane = threadIdx.x & 63;
  const int w = __builtin_amdgcn_readfirstlane(threadIdx.x >> 6);
  const int hd = w & 1;
  const int b = blockIdx.x * 4 + (w >> 1);
  const unsigned lds0 = (unsigned)(uintptr_t)lds;
  const int r = lane & 31, h = lane >> 5;

  {
    const unsigned wo = w * 1024 + lane * 16;
    glds16(wq, wo, lds0 + w * 1024);
    glds16(wk, wo, lds0 + 8192 + w * 1024);
    glds16(wv, wo, lds0 + 16384 + w * 1024);
    const unsigned ci = w * 64 + lane;
    glds16(wr, ((ci >> 2) & 63) * 128 + (ci >> 8) * 64 + (ci & 3) * 16, lds0 + 24576 + w * 1024);
  }
  const unsigned ebase = lds0 + W_BYTES + w * E_BYTES;
  {
    const char* eb = e + (size_t)b * (NF * ND * 2);
    const unsigned vE = (lane >> 3) * 128 + (((lane & 7) ^ (lane >> 4)) * 16);
    const unsigned vO = vE ^ 64;
#pragma unroll
    for (int n = 0; n < 12; ++n) glds16(eb + n * 1024, (n & 1) ? vO : vE, ebase + n * 1024);
    if (lane < 32) glds16(eb + 12 * 1024, vE, ebase + 12 * 1024);
  }
  asm volatile("s_waitcnt vmcnt(0)\n\ts_barrier" ::: "memory");
  const unsigned long long rt1 = __builtin_amdgcn_s_memrealtime();
  const int t16 = lane & 15, beta = (lane >> 4) & 1, p4 = t16 & 3, q4 = t16 >> 2;
  const lds_cptr wp = (lds_cptr)lds + hd * 4096 + (8 * h + q4) * 64 + (16 * (p4 & 1) + 8 * beta + 4 * (p4 >> 1)) * 2;
  const lds_cptr ep = (lds_cptr)lds + W_BYTES + w * E_BYTES;
  int eoff[4], eoff3[4];
  const int rho = 2 * ((r >> 2) & 1) + (r & 1);
#pragma unroll
  for (int s = 0; s < 4; ++s) {
    eoff[s] = r * 128 + 16 * ((2 * s + h) ^ ((r >> 1) & 7));
    eoff3[s] = (96 + rho) * 128 + 16 * ((2 * s + h) ^ (rho >> 1));
  }
  const f32x16 z16 = {};

  bf16x8 kf[4][2], vf[4][2];
  {
    bf16x8 wkf[4], wvf[4];
#pragma unroll
    for (int s = 0; s < 4; ++s) { wkf[s] = wfrag(wp, 8192 + s * 1024); wvf[s] = wfrag(wp, 16384 + s * 1024); }
#pragma unroll
    for (int T = 0; T < 4; ++T) {
      bf16x8 ef[4];
#pragma unroll
      for (int s = 0; s < 4; ++s) ef[s] = lds16(ep + (T < 3 ? eoff[s] + T * 4096 : eoff3[s]));
      f32x16 ka = z16, va = z16;
#pragma unroll
      for (int s = 0; s < 4; ++s) { ka = MFMA32(wkf[s], ef[s], ka); va = MFMA32(ef[s], wvf[s], va); }
      kf[T][0] = pack8(ka, 0); kf[T][1] = pack8(ka, 1);
      vf[T][0] = pack8(va, 0); if (T < 3) vf[T][1] = pack8(va, 1);
    }
  }
  bf16x8 wqf[4], wrf[4];
#pragma unroll
  for (int s = 0; s < 4; ++s) { wqf[s] = wfrag(wp, s * 1024); wrf[s] = wfrag(wp, 24576 + s * 1024); }
  bf16x8 ones;
  { const unsigned o = (((t16 >> 2) & 1) == beta) ? 0x3f803f80u : 0u; u32x4 o4 = {o, o, o, o}; ones = __builtin_bit_cast(bf16x8, o4); }

  char* ob = out + (size_t)b * (NF * ND * 2) + hd * 64 + h * 32;
#pragma unroll 1
  for (int Tq = 0; Tq < 4; ++Tq) {
    bf16x8 ef[4];
#pragma unroll
    for (int s = 0; s < 4; ++s) ef[s] = lds16(ep + (Tq < 3 ? eoff[s] + Tq * 4096 : eoff3[s]));
    f32x16 qa = z16, ra = z16;
#pragma unroll
    for (int s = 0; s < 4; ++s) { qa = MFMA32(wqf[s], ef[s], qa); ra = MFMA32(wrf[s], ef[s], ra); }
    const bf16x8 q0 = pack8(qa, 0), q1 = pack8(qa, 1);
    f32x16 sc[4];
#pragma unroll
    for (int T = 0; T < 4; ++T) { sc[T] = MFMA32(kf[T][0], q0, z16); sc[T] = MFMA32(kf[T][1], q1, sc[T]); }
    float m0 = __builtin_fmaxf(sc[3][0], sc[3][1]), m1 = __builtin_fmaxf(sc[0][0], sc[0][1]);
#pragma unroll
    for (int i = 2; i < 16; i += 2) m1 = MX3(m1, sc[0][i], sc[0][i + 1]);
#pragma unroll
    for (int i = 0; i < 16; i += 2) { m0 = MX3(m0, sc[1][i], sc[1][i + 1]); m1 = MX3(m1, sc[2][i], sc[2][i + 1]); }
    float m = __builtin_fmaxf(m0, m1);
    { auto rr = __builtin_amdgcn_permlane32_swap(__float_as_uint(m), __float_as_uint(m), false, false);
      m = __builtin_fmaxf(__uint_as_float(rr[0]), __uint_as_float(rr[1])); }
    const float nm = -m * CSCALE;
    float cs = CSCALE; asm volatile("" : "+v"(cs));
    f32x16 oa = z16; f32x4 la = {};
#pragma unroll
    for (int T = 0; T < 3; ++T) {
#pragma unroll
      for (int i = 0; i < 16; ++i) sc[T][i] = __builtin_amdgcn_exp2f(__builtin_fmaf(sc[T][i], cs, nm));
      const bf16x8 p0 = pack8(sc[T], 0), p1 = pack8(sc[T], 1);
      oa = MFMA32(vf[T][0], p0, oa); la = MFMA16(ones, p0, la);
      oa = MFMA32(vf[T][1], p1, oa); la = MFMA16(ones, p1, la);
    }
    {
      const float e0 = __builtin_amdgcn_exp2f(__builtin_fmaf(sc[3][0], cs, nm)), e1 = __builtin_amdgcn_exp2f(__builtin_fmaf(sc[3][1], cs, nm));
      u32x4 p = {pk2(e0, e1), 0u, 0u, 0u};
      const bf16x8 p0 = __builtin_bit_cast(bf16x8, p);
      oa = MFMA32(vf[3][0], p0, oa); la = MFMA16(ones, p0, la);
    }
    const float rl = __builtin_amdgcn_rcpf(la[0]);
    unsigned o[8];
#pragma unroll
    for (int i = 0; i < 8; ++i) {
      const unsigned u = pk2(__builtin_fmaf(oa[2 * i], rl, ra[2 * i]), __builtin_fmaf(oa[2 * i + 1], rl, ra[2 * i + 1]));
      const s16x2 zz = {0, 0};
      o[i] = __builtin_bit_cast(unsigned, __builtin_elementwise_max(__builtin_bit_cast(s16x2, u), zz));
    }
    const int row = Tq < 3 ? Tq * 32 + r : 96 + rho;
    if (Tq < 3 || (r & ~5) == 0) {
      u32x4 o0 = {o[0], o[1], o[2], o[3]}, o1 = {o[4], o[5], o[6], o[7]};
      asm volatile("global_store_dwordx4 %0, %1, off nt\n\tglobal_store_dwordx4 %0, %2, off offset:16 nt\n\ts_nop 1" :: "v"(ob + row * 128), "v"(o0), "v"(o1) : "memory");
    }
  }
  { unsigned long long tgt = rt0 + 3000 + 0 * rt1; while (__builtin_amdgcn_s_memrealtime() < tgt) __builtin_amdgcn_s_sleep(2);
    const unsigned long long mn = __hip_atomic_load(gmin, __ATOMIC_RELAXED, __HIP_MEMORY_SCOPE_AGENT);
    tgt = mn + 4000 + 0 * (rt0 - mn); while (__builtin_amdgcn_s_memrealtime() < tgt) __builtin_amdgcn_s_sleep(2); }
}

extern "C" void kernel_launch(void* const* d_in, const int* in_sizes, int n_in, void* d_out, int out_size, void* d_ws, size_t ws_size,
                              hipStream_t stream) {
  (void)in_sizes; (void)n_in; (void)out_size; (void)d_ws; (void)ws_size;
  hipFuncSetAttribute((const void*)autoint_kernel, hipFuncAttributeMaxDynamicSharedMemorySize, LDS_BYTES);
  hipMemsetAsync(d_ws, 0xff, 16, stream);
  autoint_kernel<<<256, 512, LDS_BYTES, stream>>>((const char*)d_in[0], (const char*)d_in[1], (const char*)d_in[2], (const char*)d_in[3],
                                                   (const char*)d_in[4], (char*)d_out, (unsigned long long*)d_ws);
}
```

```cpp
#include <hip/hip_runtime.h>
#include <hip/hip_bf16.h>
#include <stdint.h>

typedef __bf16 bf16x8 __attribute__((ext_vector_type(8)));
typedef __bf16 bf16x2 __attribute__((ext_vector_type(2)));
typedef short s16x4 __attribute__((ext_vector_type(4)));
typedef short s16x2 __attribute__((ext_vector_type(2)));
typedef float f32x16 __attribute__((ext_vector_type(16)));
typedef float f32x4 __attribute__((ext_vector_type(4)));
typedef float f32x2 __attribute__((ext_vector_type(2)));
typedef unsigned u32x4 __attribute__((ext_vector_type(4)));
typedef unsigned u32x2 __attribute__((ext_vector_type(2)));
typedef __attribute__((address_space(3))) const char* lds_cptr;
typedef __attribute__((address_space(3))) char* lds_ptr;

#define DI __device__ __forceinline__
#define MFMA32(a, b, c) __builtin_amdgcn_mfma_f32_32x32x16_bf16((a), (b), (c), 0, 0, 0)
#define MFMA16(a, b, c) __builtin_amdgcn_mfma_f32_16x16x32_bf16((a), (b), (c), 0, 0, 0)

constexpr int NF = 100, ND = 64;
constexpr int W_BYTES = 32768, E_BYTES = 104 * 128, LDS_BYTES = W_BYTES + 8 * E_BYTES;
constexpr float CSCALE = 0.17677669529663687f * 1.4426950408889634f;

DI unsigned pk2(float lo, float hi) {
  f32x2 v = {lo, hi};
  return __builtin_bit_cast(unsigned, __builtin_convertvector(v, bf16x2));
}
DI bf16x8 pack8(const f32x16& x, int s) {
  u32x4 p = {pk2(x[8 * s], x[8 * s + 1]), pk2(x[8 * s + 2], x[8 * s + 3]), pk2(x[8 * s + 4], x[8 * s + 5]), pk2(x[8 * s + 6], x[8 * s + 7])};
  return __builtin_bit_cast(bf16x8, p);
}
DI void glds16(const char* sbase, unsigned voff, unsigned lds_base) {
  asm volatile("s_mov_b32 m0, %2\n\ts_nop 0\n\tglobal_load_lds_dwordx4 %0, %1" ::"v"(voff), "s"(sbase), "s"(lds_base) : "memory");
}
DI s16x4 vtr(lds_cptr p) { return __builtin_amdgcn_ds_read_tr16_b64_v4i16((__attribute__((address_space(3))) s16x4*)p); }
DI bf16x8 wfrag(lds_cptr p, int off) {
  s16x4 lo = vtr(p + off), hi = vtr(p + off + 256);
  return __builtin_bit_cast(bf16x8, __builtin_shufflevector(lo, hi, 0, 1, 2, 3, 4, 5, 6, 7));
}
DI bf16x8 lds16(lds_cptr p) { return *(const __attribute__((address_space(3))) bf16x8*)p; }
#define MX3(a, b, c) __builtin_fmaxf(__builtin_fmaxf((a), (b)), (c))

__global__ __launch_bounds__(512, 2) void autoint_kernel(const char* __restrict__ e, const char* __restrict__ wq, const char* __restrict__ wk,
                                                         const char* __restrict__ wv, const char* __restrict__ wr, char* __restrict__ out) {
  unsigned long long rt0; asm volatile("s_memrealtime %0" : "=s"(rt0) :: "memory");
  unsigned long long rta; asm volatile("s_memrealtime %0\n\ts_waitcnt lgkmcnt(0)" : "=s"(rta) : "s"(e), "s"(wq), "s"(out) : "memory");
  extern __shared__ __attribute__((aligned(16))) char lds[];
  const int lane = threadIdx.x & 63;
  const int w = __builtin_amdgcn_readfirstlane(threadIdx.x >> 6);
  const int hd = w & 1;
  const int b = blockIdx.x * 4 + (w >> 1);
  const unsigned lds0 = (unsigned)(uintptr_t)lds;
  const int r = lane & 31, h = lane >> 5;

  {
    const unsigned wo = w * 1024 + lane * 16;
    glds16(wq, wo, lds0 + w * 1024);
    glds16(wk, wo, lds0 + 8192 + w * 1024);
    glds16(wv, wo, lds0 + 16384 + w * 1024);
    const unsigned ci = w * 64 + lane;
    glds16(wr, ((ci >> 2) & 63) * 128 + (ci >> 8) * 64 + (ci & 3) * 16, lds0 + 24576 + w * 1024);
  }
  const unsigned ebase = lds0 + W_BYTES + w * E_BYTES;
  {
    const char* eb = e + (size_t)b * (NF * ND * 2);
    const unsigned vE = (lane >> 3) * 128 + (((lane & 7) ^ (lane >> 4)) * 16);
    const unsigned vO = vE ^ 64;
#pragma unroll
    for (int n = 0; n < 12; ++n) glds16(eb + n * 1024, (n & 1) ? vO : vE, ebase + n * 1024);
    if (lane < 32) glds16(eb + 12 * 1024, vE, ebase + 12 * 1024);
  }
  asm volatile("s_waitcnt vmcnt(0)\n\ts_barrier" ::: "memory");
  const unsigned long long rt1 = __builtin_amdgcn_s_memrealtime();
  const int t16 = lane & 15, beta = (lane >> 4) & 1, p4 = t16 & 3, q4 = t16 >> 2;
  const lds_cptr wp = (lds_cptr)lds + hd * 4096 + (8 * h + q4) * 64 + (16 * (p4 & 1) + 8 * beta + 4 * (p4 >> 1)) * 2;
  const lds_cptr ep = (lds_cptr)lds + W_BYTES + w * E_BYTES;
  int eoff[4], eoff3[4];
  const int rho = 2 * ((r >> 2) & 1) + (r & 1);
#pragma unroll
  for (int s = 0; s < 4; ++s) {
    eoff[s] = r * 128 + 16 * ((2 * s + h) ^ ((r >> 1) & 7));
    eoff3[s] = (96 + rho) * 128 + 16 * ((2 * s + h) ^ (rho >> 1));
  }
  const f32x16 z16 = {};

  bf16x8 kf[4][2], vf[4][2];
  {
    bf16x8 wkf[4], wvf[4];
#pragma unroll
    for (int s = 0; s < 4; ++s) { wkf[s] = wfrag(wp, 8192 + s * 1024); wvf[s] = wfrag(wp, 16384 + s * 1024); }
#pragma unroll
    for (int T = 0; T < 4; ++T) {
      bf16x8 ef[4];
#pragma unroll
      for (int s = 0; s < 4; ++s) ef[s] = lds16(ep + (T < 3 ? eoff[s] + T * 4096 : eoff3[s]));
      f32x16 ka = z16, va = z16;
#pragma unroll
      for (int s = 0; s < 4; ++s) { ka = MFMA32(wkf[s], ef[s], ka); va = MFMA32(ef[s], wvf[s], va); }
      kf[T][0] = pack8(ka, 0); kf[T][1] = pack8(ka, 1);
      vf[T][0] = pack8(va, 0); if (T < 3) vf[T][1] = pack8(va, 1);
    }
  }
  bf16x8 wqf[4], wrf[4];
#pragma unroll
  for (int s = 0; s < 4; ++s) { wqf[s] = wfrag(wp, s * 1024); wrf[s] = wfrag(wp, 24576 + s * 1024); }
  bf16x8 ones;
  { const unsigned o = (((t16 >> 2) & 1) == beta) ? 0x3f803f80u : 0u; u32x4 o4 = {o, o, o, o}; ones = __builtin_bit_cast(bf16x8, o4); }

  char* ob = out + (size_t)b * (NF * ND * 2) + hd * 64 + h * 32;
#pragma unroll 1
  for (int Tq = 0; Tq < 4; ++Tq) {
    bf16x8 ef[4];
#pragma unroll
    for (int s = 0; s < 4; ++s) ef[s] = lds16(ep + (Tq < 3 ? eoff[s] + Tq * 4096 : eoff3[s]));
    f32x16 qa = z16, ra = z16;
#pragma unroll
    for (int s = 0; s < 4; ++s) { qa = MFMA32(wqf[s], ef[s], qa); ra = MFMA32(wrf[s], ef[s], ra); }
    const bf16x8 q0 = pack8(qa, 0), q1 = pack8(qa, 1);
    f32x16 sc[4];
#pragma unroll
    for (int T = 0; T < 4; ++T) { sc[T] = MFMA32(kf[T][0], q0, z16); sc[T] = MFMA32(kf[T][1], q1, sc[T]); }
    float m0 = __builtin_fmaxf(sc[3][0], sc[3][1]), m1 = __builtin_fmaxf(sc[0][0], sc[0][1]);
#pragma unroll
    for (int i = 2; i < 16; i += 2) m1 = MX3(m1, sc[0][i], sc[0][i + 1]);
#pragma unroll
    for (int i = 0; i < 16; i += 2) { m0 = MX3(m0, sc[1][i], sc[1][i + 1]); m1 = MX3(m1, sc[2][i], sc[2][i + 1]); }
    float m = __builtin_fmaxf(m0, m1);
    { auto rr = __builtin_amdgcn_permlane32_swap(__float_as_uint(m), __float_as_uint(m), false, false);
      m = __builtin_fmaxf(__uint_as_float(rr[0]), __uint_as_float(rr[1])); }
    const float nm = -m * CSCALE;
    float cs = CSCALE; asm volatile("" : "+v"(cs));
    f32x16 oa = z16; f32x4 la = {};
#pragma unroll
    for (int T = 0; T < 3; ++T) {
#pragma unroll
      for (int i = 0; i < 16; ++i) sc[T][i] = __builtin_amdgcn_exp2f(__builtin_fmaf(sc[T][i], cs, nm));
      const bf16x8 p0 = pack8(sc[T], 0), p1 = pack8(sc[T], 1);
      oa = MFMA32(vf[T][0], p0, oa); la = MFMA16(ones, p0, la);
      oa = MFMA32(vf[T][1], p1, oa); la = MFMA16(ones, p1, la);
    }
    {
      const float e0 = __builtin_amdgcn_exp2f(__builtin_fmaf(sc[3][0], cs, nm)), e1 = __builtin_amdgcn_exp2f(__builtin_fmaf(sc[3][1], cs, nm));
      u32x4 p = {pk2(e0, e1), 0u, 0u, 0u};
      const bf16x8 p0 = __builtin_bit_cast(bf16x8, p);
      oa = MFMA32(vf[3][0], p0, oa); la = MFMA16(ones, p0, la);
    }
    const float rl = __builtin_amdgcn_rcpf(la[0]);
    unsigned o[8];
#pragma unroll
    for (int i = 0; i < 8; ++i) {
      const unsigned u = pk2(__builtin_fmaf(oa[2 * i], rl, ra[2 * i]), __builtin_fmaf(oa[2 * i + 1], rl, ra[2 * i + 1]));
      const s16x2 zz = {0, 0};
      o[i] = __builtin_bit_cast(unsigned, __builtin_elementwise_max(__builtin_bit_cast(s16x2, u), zz));
    }
    const int row = Tq < 3 ? Tq * 32 + r : 96 + rho;
    if (Tq < 3 || (r & ~5) == 0) {
      u32x4 o0 = {o[0], o[1], o[2], o[3]}, o1 = {o[4], o[5], o[6], o[7]};
      asm volatile("global_store_dwordx4 %0, %1, off nt\n\tglobal_store_dwordx4 %0, %2, off offset:16 nt\n\ts_nop 1" :: "v"(ob + row * 128), "v"(o0), "v"(o1) : "memory");
    }
  }
  { const unsigned long long tgt = rt0 + 4000 + 32 * (rta - rt0) + 0 * rt1; while (__builtin_amdgcn_s_memrealtime() < tgt) __builtin_amdgcn_s_sleep(2); }
}

extern "C" void kernel_launch(void* const* d_in, const int* in_sizes, int n_in, void* d_out, int out_size, void* d_ws, size_t ws_size,
                              hipStream_t stream) {
  (void)in_sizes; (void)n_in; (void)out_size; (void)d_ws; (void)ws_size;
  hipFuncSetAttribute((const void*)autoint_kernel, hipFuncAttributeMaxDynamicSharedMemorySize, LDS_BYTES);
  autoint_kernel<<<256, 512, LDS_BYTES, stream>>>((const char*)d_in[0], (const char*)d_in[1], (const char*)d_in[2], (const char*)d_in[3],
                                                   (const char*)d_in[4], (char*)d_out);
}
```

```cpp
#include <hip/hip_runtime.h>
#include <hip/hip_bf16.h>
#include <stdint.h>

typedef __bf16 bf16x8 __attribute__((ext_vector_type(8)));
typedef __bf16 bf16x2 __attribute__((ext_vector_type(2)));
typedef short s16x4 __attribute__((ext_vector_type(4)));
typedef short s16x2 __attribute__((ext_vector_type(2)));
typedef float f32x16 __attribute__((ext_vector_type(16)));
typedef float f32x4 __attribute__((ext_vector_type(4)));
typedef float f32x2 __attribute__((ext_vector_type(2)));
typedef unsigned u32x4 __attribute__((ext_vector_type(4)));
typedef unsigned u32x2 __attribute__((ext_vector_type(2)));
typedef __attribute__((address_space(3))) const char* lds_cptr;
typedef __attribute__((address_space(3))) char* lds_ptr;

#define STORE(v, p) __builtin_nontemporal_store((v), (p))
#define DI __device__ __forceinline__
#define MFMA32(a, b, c) __builtin_amdgcn_mfma_f32_32x32x16_bf16((a), (b), (c), 0, 0, 0)
#define MFMA16(a, b, c) __builtin_amdgcn_mfma_f32_16x16x32_bf16((a), (b), (c), 0, 0, 0)

constexpr int NF = 100, ND = 64;
constexpr int W_BYTES = 32768, E_BYTES = 104 * 128, LDS_BYTES = W_BYTES + 8 * E_BYTES + 8 * 2048;
constexpr float CSCALE = 0.17677669529663687f * 1.4426950408889634f;

DI unsigned pk2(float lo, float hi) {
  f32x2 v = {lo, hi};
  return __builtin_bit_cast(unsigned, __builtin_convertvector(v, bf16x2));
}
DI bf16x8 pack8(const f32x16& x, int s) {
  u32x4 p = {pk2(x[8 * s], x[8 * s + 1]), pk2(x[8 * s + 2], x[8 * s + 3]), pk2(x[8 * s + 4], x[8 * s + 5]), pk2(x[8 * s + 6], x[8 * s + 7])};
  return __builtin_bit_cast(bf16x8, p);
}
DI void glds16(const char* sbase, unsigned voff, unsigned lds_base) {
  asm volatile("s_mov_b32 m0, %2\n\ts_nop 0\n\tglobal_load_lds_dwordx4 %0, %1" ::"v"(voff), "s"(sbase), "s"(lds_base) : "memory");
}
DI s16x4 vtr(lds_cptr p) { return __builtin_amdgcn_ds_read_tr16_b64_v4i16((__attribute__((address_space(3))) s16x4*)p); }
DI bf16x8 wfrag(lds_cptr p, int off) {
  s16x4 lo = vtr(p + off), hi = vtr(p + off + 256);
  return __builtin_bit_cast(bf16x8, __builtin_shufflevector(lo, hi, 0, 1, 2, 3, 4, 5, 6, 7));
}
DI bf16x8 lds16(lds_cptr p) { return *(const __attribute__((address_space(3))) bf16x8*)p; }
#define MX3(a, b, c) __builtin_fmaxf(__builtin_fmaxf((a), (b)), (c))

__global__ __launch_bounds__(512, 2) void autoint_kernel(const char* __restrict__ e, const char* __restrict__ wq, const char* __restrict__ wk,
                                                         const char* __restrict__ wv, const char* __restrict__ wr, char* __restrict__ out) {
  extern __shared__ __attribute__((aligned(16))) char lds[];
  const int lane = threadIdx.x & 63;
  const int w = __builtin_amdgcn_readfirstlane(threadIdx.x >> 6);
  const int hd = w & 1;
  const int b = blockIdx.x * 4 + (w >> 1);
  const unsigned lds0 = (unsigned)(uintptr_t)lds;
  const int r = lane & 31, h = lane >> 5;

  {
    const unsigned wo = w * 1024 + lane * 16;
    glds16(wq, wo, lds0 + w * 1024);
    glds16(wk, wo, lds0 + 8192 + w * 1024);
    glds16(wv, wo, lds0 + 16384 + w * 1024);
    const unsigned ci = w * 64 + lane;
    glds16(wr, ((ci >> 2) & 63) * 128 + (ci >> 8) * 64 + (ci & 3) * 16, lds0 + 24576 + w * 1024);
  }
  const unsigned ebase = lds0 + W_BYTES + w * E_BYTES;
  {
    const char* eb = e + (size_t)b * (NF * ND * 2);
    const unsigned vE = (lane >> 3) * 128 + (((lane & 7) ^ (lane >> 4)) * 16);
    const unsigned vO = vE ^ 64;
#pragma unroll
    for (int n = 0; n < 12; ++n) glds16(eb + n * 1024, (n & 1) ? vO : vE, ebase + n * 1024);
    if (lane < 32) glds16(eb + 12 * 1024, vE, ebase + 12 * 1024);
  }
  asm volatile("s_waitcnt vmcnt(0)\n\ts_barrier" ::: "memory");

  const int t16 = lane & 15, beta = (lane >> 4) & 1, p4 = t16 & 3, q4 = t16 >> 2;
  const lds_cptr wp = (lds_cptr)lds + hd * 4096 + (8 * h + q4) * 64 + (16 * (p4 & 1) + 8 * beta + 4 * (p4 >> 1)) * 2;
  const lds_cptr ep = (lds_cptr)lds + W_BYTES + w * E_BYTES;
  int eoff[4], eoff3[4];
  const int rho = 2 * ((r >> 2) & 1) + (r & 1);
#pragma unroll
  for (int s = 0; s < 4; ++s) {
    eoff[s] = r * 128 + 16 * ((2 * s + h) ^ ((r >> 1) & 7));
    eoff3[s] = (96 + rho) * 128 + 16 * ((2 * s + h) ^ (rho >> 1));
  }
  const f32x16 z16 = {};

  bf16x8 kf[4][2], vf[4][2];
  {
    bf16x8 wkf[4], wvf[4];
#pragma unroll
    for (int s = 0; s < 4; ++s) { wkf[s] = wfrag(wp, 8192 + s * 1024); wvf[s] = wfrag(wp, 16384 + s * 1024); }
#pragma unroll
    for (int T = 0; T < 4; ++T) {
      bf16x8 ef[4];
#pragma unroll
      for (int s = 0; s < 4; ++s) ef[s] = lds16(ep + (T < 3 ? eoff[s] + T * 4096 : eoff3[s]));
      f32x16 ka = z16, va = z16;
#pragma unroll
      for (int s = 0; s < 4; ++s) { ka = MFMA32(wkf[s], ef[s], ka); va = MFMA32(ef[s], wvf[s], va); }
      kf[T][0] = pack8(ka, 0); kf[T][1] = pack8(ka, 1);
      vf[T][0] = pack8(va, 0); if (T < 3) vf[T][1] = pack8(va, 1);
    }
  }
  bf16x8 wqf[4], wrf[4];
#pragma unroll
  for (int s = 0; s < 4; ++s) { wqf[s] = wfrag(wp, s * 1024); wrf[s] = wfrag(wp, 24576 + s * 1024); }
  bf16x8 ones;
  { const unsigned o = (((t16 >> 2) & 1) == beta) ? 0x3f803f80u : 0u; u32x4 o4 = {o, o, o, o}; ones = __builtin_bit_cast(bf16x8, o4); }

  char* ob = out + (size_t)b * (NF * ND * 2) + hd * 64 + h * 32;
#pragma unroll 1
  for (int Tq = 0; Tq < 4; ++Tq) {
    bf16x8 ef[4];
#pragma unroll
    for (int s = 0; s < 4; ++s) ef[s] = lds16(ep + (Tq < 3 ? eoff[s] + Tq * 4096 : eoff3[s]));
    f32x16 qa = z16, ra = z16;
#pragma unroll
    for (int s = 0; s < 4; ++s) { qa = MFMA32(wqf[s], ef[s], qa); ra = MFMA32(wrf[s], ef[s], ra); }
    const bf16x8 q0 = pack8(qa, 0), q1 = pack8(qa, 1);
    f32x16 sc[4];
#pragma unroll
    for (int T = 0; T < 4; ++T) { sc[T] = MFMA32(kf[T][0], q0, z16); sc[T] = MFMA32(kf[T][1], q1, sc[T]); }
    float m0 = __builtin_fmaxf(sc[3][0], sc[3][1]), m1 = __builtin_fmaxf(sc[0][0], sc[0][1]);
#pragma unroll
    for (int i = 2; i < 16; i += 2) m1 = MX3(m1, sc[0][i], sc[0][i + 1]);
#pragma unroll
    for (int i = 0; i < 16; i += 2) { m0 = MX3(m0, sc[1][i], sc[1][i + 1]); m1 = MX3(m1, sc[2][i], sc[2][i + 1]); }
    float m = __builtin_fmaxf(m0, m1);
    { auto rr = __builtin_amdgcn_permlane32_swap(__float_as_uint(m), __float_as_uint(m), false, false);
      m = __builtin_fmaxf(__uint_as_float(rr[0]), __uint_as_float(rr[1])); }
    const float nm = -m * CSCALE;
    float cs = CSCALE; asm volatile("" : "+v"(cs));
    f32x16 oa = z16; f32x4 la = {};
#pragma unroll
    for (int T = 0; T < 3; ++T) {
#pragma unroll
      for (int i = 0; i < 16; ++i) sc[T][i] = __builtin_amdgcn_exp2f(__builtin_fmaf(sc[T][i], cs, nm));
      const bf16x8 p0 = pack8(sc[T], 0), p1 = pack8(sc[T], 1);
      oa = MFMA32(vf[T][0], p0, oa); la = MFMA16(ones, p0, la);
      oa = MFMA32(vf[T][1], p1, oa); la = MFMA16(ones, p1, la);
    }
    {
      const float e0 = __builtin_amdgcn_exp2f(__builtin_fmaf(sc[3][0], cs, nm)), e1 = __builtin_amdgcn_exp2f(__builtin_fmaf(sc[3][1], cs, nm));
      u32x4 p = {pk2(e0, e1), 0u, 0u, 0u};
      const bf16x8 p0 = __builtin_bit_cast(bf16x8, p);
      oa = MFMA32(vf[3][0], p0, oa); la = MFMA16(ones, p0, la);
    }
    const float rl = __builtin_amdgcn_rcpf(la[0]);
    unsigned o[8];
#pragma unroll
    for (int i = 0; i < 8; ++i) {
      const unsigned u = pk2(__builtin_fmaf(oa[2 * i], rl, ra[2 * i]), __builtin_fmaf(oa[2 * i + 1], rl, ra[2 * i + 1]));
      const s16x2 zz = {0, 0};
      o[i] = __builtin_bit_cast(unsigned, __builtin_elementwise_max(__builtin_bit_cast(s16x2, u), zz));
    }
    {
      u32x4 o0 = {o[0], o[1], o[2], o[3]}, o1 = {o[4], o[5], o[6], o[7]};
      lds_ptr stg = (lds_ptr)lds + W_BYTES + 8 * E_BYTES + w * 2048;
      const int wa = r * 64 + 16 * ((2 * h) ^ ((r >> 1) & 3));
      *(__attribute__((address_space(3))) u32x4*)(stg + wa) = o0;
      *(__attribute__((address_space(3))) u32x4*)(stg + (wa ^ 16)) = o1;
      const int rr = lane >> 2, cc = lane & 3;
      const int ra_ = rr * 64 + 16 * (cc ^ ((rr >> 1) & 3));
      u32x4 a0 = *(__attribute__((address_space(3))) u32x4*)(stg + ra_);
      u32x4 a1 = *(__attribute__((address_space(3))) u32x4*)(stg + ra_ + 1024);
      char* ob2 = out + (size_t)b * (NF * ND * 2) + hd * 64 + cc * 16;
      if (Tq < 3) {
        STORE(a0, (u32x4*)(ob2 + (Tq * 32 + rr) * 128));
        STORE(a1, (u32x4*)(ob2 + (Tq * 32 + 16 + rr) * 128));
      } else if ((rr & ~5) == 0) {
        STORE(a0, (u32x4*)(ob2 + (96 + 2 * (rr >> 2) + (rr & 1)) * 128));
      }
    }
  }
}

extern "C" void kernel_launch(void* const* d_in, const int* in_sizes, int n_in, void* d_out, int out_size, void* d_ws, size_t ws_size,
                              hipStream_t stream) {
  (void)in_sizes; (void)n_in; (void)out_size; (void)d_ws; (void)ws_size;
  hipFuncSetAttribute((const void*)autoint_kernel, hipFuncAttributeMaxDynamicSharedMemorySize, LDS_BYTES);
  autoint_kernel<<<256, 512, LDS_BYTES, stream>>>((const char*)d_in[0], (const char*)d_in[1], (const char*)d_in[2], (const char*)d_in[3],
                                                   (const char*)d_in[4], (char*)d_out);
}
```

```cpp
#include <hip/hip_runtime.h>
#include <hip/hip_bf16.h>
#include <stdint.h>

typedef __bf16 bf16x8 __attribute__((ext_vector_type(8)));
typedef __bf16 bf16x2 __attribute__((ext_vector_type(2)));
typedef short s16x4 __attribute__((ext_vector_type(4)));
typedef short s16x2 __attribute__((ext_vector_type(2)));
typedef float f32x16 __attribute__((ext_vector_type(16)));
typedef float f32x4 __attribute__((ext_vector_type(4)));
typedef float f32x2 __attribute__((ext_vector_type(2)));
typedef unsigned u32x4 __attribute__((ext_vector_type(4)));
typedef __attribute__((address_space(3))) const char* lds_cptr;

#define DI __device__ __forceinline__
#define MFMA32(a, b, c) __builtin_amdgcn_mfma_f32_32x32x16_bf16((a), (b), (c), 0, 0, 0)
#define MFMA16(a, b, c) __builtin_amdgcn_mfma_f32_16x16x32_bf16((a), (b), (c), 0, 0, 0)
#define MX3(a, b, c) __builtin_fmaxf(__builtin_fmaxf((a), (b)), (c))

constexpr int W_BYTES = 32768, E_BYTES = 13312, LDS_BYTES = 99328;

DI unsigned pk2(float lo, float hi) {
  f32x2 v = {lo, hi};
  return __builtin_bit_cast(unsigned, __builtin_convertvector(v, bf16x2));
}
DI bf16x8 pack8(const f32x16& x, int s) {
  u32x4 p = {pk2(x[8 * s], x[8 * s + 1]), pk2(x[8 * s + 2], x[8 * s + 3]), pk2(x[8 * s + 4], x[8 * s + 5]), pk2(x[8 * s + 6], x[8 * s + 7])};
  return __builtin_bit_cast(bf16x8, p);
}
DI s16x4 vtr(lds_cptr p) { return __builtin_amdgcn_ds_read_tr16_b64_v4i16((__attribute__((address_space(3))) s16x4*)p); }
DI bf16x8 wfrag(lds_cptr p, int off) {
  s16x4 lo = vtr(p + off), hi = vtr(p + off + 256);
  return __builtin_bit_cast(bf16x8, __builtin_shufflevector(lo, hi, 0, 1, 2, 3, 4, 5, 6, 7));
}
DI bf16x8 lds16(lds_cptr p) { return *(const __attribute__((address_space(3))) bf16x8*)p; }

DI void safe_path(lds_cptr lds, int w, int lb, int lane, char* ob) {
  const int hd = w & 1, r = lane & 31, h = lane >> 5;
  const int t16 = lane & 15, beta = (lane >> 4) & 1, p4 = t16 & 3, q4 = t16 >> 2;
  const lds_cptr wp = lds + hd * 4096 + (8 * h + q4) * 64 + (16 * (p4 & 1) + 8 * beta + 4 * (p4 >> 1)) * 2;
  const lds_cptr ep = lds + W_BYTES + lb * E_BYTES;
  int eoff[4], eoff3[4];
  const int rho = 2 * ((r >> 2) & 1) + (r & 1);
#pragma unroll
  for (int s = 0; s < 4; ++s) {
    eoff[s] = r * 128 + 16 * ((2 * s + h) ^ ((r >> 1) & 7));
    eoff3[s] = (96 + rho) * 128 + 16 * ((2 * s + h) ^ (rho >> 1));
  }
  const f32x16 z16 = {};
  bf16x8 kf[4][2], vf[4][2];
  {
    bf16x8 wkf[4], wvf[4];
#pragma unroll
    for (int s = 0; s < 4; ++s) { wkf[s] = wfrag(wp, 8192 + s * 1024); wvf[s] = wfrag(wp, 16384 + s * 1024); }
#pragma unroll
    for (int T = 0; T < 4; ++T) {
      bf16x8 ef[4];
#pragma unroll
      for (int s = 0; s < 4; ++s) ef[s] = lds16(ep + (T < 3 ? eoff[s] + T * 4096 : eoff3[s]));
      f32x16 ka = z16, va = z16;
#pragma unroll
      for (int s = 0; s < 4; ++s) { ka = MFMA32(wkf[s], ef[s], ka); va = MFMA32(ef[s], wvf[s], va); }
      kf[T][0] = pack8(ka, 0); kf[T][1] = pack8(ka, 1);
      vf[T][0] = pack8(va, 0); if (T < 3) vf[T][1] = pack8(va, 1);
    }
  }
  bf16x8 wqf[4], wrf[4];
#pragma unroll
  for (int s = 0; s < 4; ++s) { wqf[s] = wfrag(wp, s * 1024); wrf[s] = wfrag(wp, 24576 + s * 1024); }
  bf16x8 ones;
  { const unsigned o = (((t16 >> 2) & 1) == beta) ? 0x3f803f80u : 0u; u32x4 o4 = {o, o, o, o}; ones = __builtin_bit_cast(bf16x8, o4); }
#pragma unroll 1
  for (int Tq = 0; Tq < 4; ++Tq) {
    bf16x8 ef[4];
#pragma unroll
    for (int s = 0; s < 4; ++s) ef[s] = lds16(ep + (Tq < 3 ? eoff[s] + Tq * 4096 : eoff3[s]));
    f32x16 qa = z16, ra = z16;
#pragma unroll
    for (int s = 0; s < 4; ++s) { qa = MFMA32(wqf[s], ef[s], qa); ra = MFMA32(wrf[s], ef[s], ra); }
    const bf16x8 q0 = pack8(qa, 0), q1 = pack8(qa, 1);
    f32x16 sc[4];
#pragma unroll
    for (int T = 0; T < 4; ++T) { sc[T] = MFMA32(kf[T][0], q0, z16); sc[T] = MFMA32(kf[T][1], q1, sc[T]); }
    float m0 = __builtin_fmaxf(sc[3][0], sc[3][1]), m1 = __builtin_fmaxf(sc[0][0], sc[0][1]);
#pragma unroll
    for (int i = 2; i < 16; i += 2) m1 = MX3(m1, sc[0][i], sc[0][i + 1]);
#pragma unroll
    for (int i = 0; i < 16; i += 2) { m0 = MX3(m0, sc[1][i], sc[1][i + 1]); m1 = MX3(m1, sc[2][i], sc[2][i + 1]); }
    float m = __builtin_fmaxf(m0, m1);
    { auto rr = __builtin_amdgcn_permlane32_swap(__float_as_uint(m), __float_as_uint(m), false, false);
      m = __builtin_fmaxf(__uint_as_float(rr[0]), __uint_as_float(rr[1])); }
    f32x16 oa = z16; f32x4 la = {};
#pragma unroll
    for (int T = 0; T < 3; ++T) {
#pragma unroll
      for (int i = 0; i < 16; ++i) sc[T][i] = __builtin_amdgcn_exp2f(sc[T][i] - m);
      const bf16x8 p0 = pack8(sc[T], 0), p1 = pack8(sc[T], 1);
      oa = MFMA32(vf[T][0], p0, oa); la = MFMA16(ones, p0, la);
      oa = MFMA32(vf[T][1], p1, oa); la = MFMA16(ones, p1, la);
    }
    {
      const float e0 = __builtin_amdgcn_exp2f(sc[3][0] - m), e1 = __builtin_amdgcn_exp2f(sc[3][1] - m);
      u32x4 p = {pk2(e0, e1), 0u, 0u, 0u};
      const bf16x8 p0 = __builtin_bit_cast(bf16x8, p);
      oa = MFMA32(vf[3][0], p0, oa); la = MFMA16(ones, p0, la);
    }
    const float rl = __builtin_amdgcn_rcpf(la[0]);
    unsigned o[8];
#pragma unroll
    for (int i = 0; i < 8; ++i) {
      const unsigned u = pk2(__builtin_fmaf(oa[2 * i], rl, ra[2 * i]), __builtin_fmaf(oa[2 * i + 1], rl, ra[2 * i + 1]));
      const s16x2 zz = {0, 0};
      o[i] = __builtin_bit_cast(unsigned, __builtin_elementwise_max(__builtin_bit_cast(s16x2, u), zz));
    }
    const int row = Tq < 3 ? Tq * 32 + r : 96 + rho;
    if (Tq < 3 || (r & ~5) == 0) {
      u32x4 o0 = {o[0], o[1], o[2], o[3]}, o1 = {o[4], o[5], o[6], o[7]};
      *(u32x4*)(ob + h * 32 + row * 128) = o0;
      *(u32x4*)(ob + h * 32 + row * 128 + 16) = o1;
    }
  }
}

__global__ __launch_bounds__(256, 1) __attribute__((aligned(4096))) void autoint_kernel(const char* __restrict__ e, const char* __restrict__ wq, const char* __restrict__ wk,
                                                         const char* __restrict__ wv, const char* __restrict__ wr, char* __restrict__ out) {
  extern __shared__ __attribute__((aligned(16))) char lds[];
  unsigned long long bad0, bad1;
  asm volatile(
      "s_load_dwordx8 s[64:71], %[ka], 0x0\n\t"
      "s_load_dwordx4 s[72:75], %[ka], 0x20\n\t"
      "s_getpc_b64 s[82:83]\n\t"
      "Lpc_%=:\n\t"
      "v_lshlrev_b32 v107, 6, %[tid]\n\t"
      "v_min_u32 v107, Lend_%=-Lpc_%=+128, v107\n\t"
      "global_load_dword a254, v107, s[82:83]\n\t"
      "v_readfirstlane_b32 s39, %[tid]\n\t"
      "v_and_b32 v2, 63, %[tid]\n\t"
      "s_lshr_b32 s38, s39, 6\n\t"
      "v_lshlrev_b32 v102, 4, v2\n\t"
      "s_lshr_b32 s57, s39, 7\n\t"
      "s_mul_i32 s40, s57, 13312\n\t"
      "s_lshl_b32 s56, %[bid], 2\n\t"
      "s_add_u32 s56, s56, s57\n\t"
      "s_mul_i32 s56, s56, 12800\n\t"
      "s_add_u32 s40, s40, 32768\n\t"
      "v_lshrrev_b32 v103, 4, v2\n\t"
      "v_xor_b32 v103, v103, v2\n\t"
      "v_and_b32 v103, 7, v103\n\t"
      "v_and_b32 v104, 0x380, v102\n\t"
      "v_lshl_or_b32 v68, v103, 4, v104\n\t"
      "v_xor_b32 v69, 64, v68\n\t"
      "s_lshl_b32 s43, s38, 11\n\t"
      "v_add_u32 v105, s43, v102\n\t"
      "s_lshl_b32 s58, s39, 5\n\t"
      "s_and_b32 s58, s58, 0x800\n\t"
      "s_add_u32 s59, s40, s58\n\t"
      "s_add_u32 s58, s58, s56\n\t"
      "v_lshlrev_b32 v106, 5, v2\n\t"
      "v_and_b32 v106, 0x780, v106\n\t"
      "v_and_or_b32 v232, v102, 48, v106\n\t"
      "s_and_b32 s42, s38, 1\n\t"
      "s_lshl_b32 s60, s42, 12\n\t"
      "s_and_b32 s42, s38, 2\n\t"
      "s_lshl_b32 s42, s42, 5\n\t"
      "s_add_u32 s42, s42, s60\n\t"
      "v_add_u32 v106, s42, v232\n\t"
      "s_add_u32 s42, s43, 8192\n\t"
      "s_mov_b32 m0, s42\n\t"
      "s_mov_b32 s54, 0x33333333\n\t"
      "s_mov_b32 s55, 0x33333333\n\t"
      "s_mul_i32 s41, s38, 3328\n\t"
      "s_add_u32 s41, s41, 86016\n\t"
      "v_mov_b32 v122, 0\n\t"
      "v_mov_b32 v123, 0\n\t"
      "v_lshrrev_b32 v116, 1, v102\n\t"
      "v_add_u32 v116, s40, v116\n\t"
      "ds_write_b64 v116, v[122:123] offset:12800\n\t"
      "ds_write_b64 v116, v[122:123] offset:39424\n\t"
      "v_and_b32 v116, 0x2c0, v102\n\t"
      "v_lshlrev_b32 v117, 5, v2\n\t"
      "v_and_or_b32 v116, v117, 32, v116\n\t"
      "v_and_or_b32 v116, v2, 16, v116\n\t"
      "v_lshlrev_b32 v118, 2, v2\n\t"
      "v_and_or_b32 v116, v118, 8, v116\n\t"
      "v_add_u32 v1, s60, v116\n\t"
      "v_lshlrev_b32 v116, 7, v2\n\t"
      "v_and_b32 v116, 0xf80, v116\n\t"
      "v_bfe_u32 v117, v2, 1, 3\n\t"
      "v_lshrrev_b32 v120, 5, v2\n\t"
      "v_xor_b32 v117, v117, v120\n\t"
      "v_lshl_or_b32 v116, v117, 4, v116\n\t"
      "v_add_u32 v3, s40, v116\n\t"
      "v_xor_b32 v152, 32, v3\n\t"
      "v_xor_b32 v224, 64, v3\n\t"
      "v_xor_b32 v225, 0x60, v3\n\t"
      "s_add_u32 s61, s40, 12288\n\t"
      "s_add_u32 s84, s41, 2048\n\t"
      "s_add_u32 s85, s40, 12928\n\t"
      "s_add_u32 s86, s41, 3088\n\t"
      "s_and_b32 s57, s39, 64\n\t"
      "s_add_u32 s57, s57, s56\n\t"
      "v_mov_b32 v116, 0x3e8293ee\n\t"
      "v_mov_b32 v117, 0x3e8293ee\n\t"
      "s_waitcnt lgkmcnt(0)\n\t"
      "global_load_dwordx4 v[108:111], v105, s[66:67]\n\t"
      "global_load_dwordx4 v[112:115], v105, s[66:67] offset:1024\n\t"
      "global_load_lds_dwordx4 v105, s[68:69]\n\t"
      "global_load_lds_dwordx4 v105, s[68:69] offset:1024\n\t"
      "s_mov_b32 m0, s59\n\t"
      "s_add_u32 s44, s64, s58\n\t"
      "s_addc_u32 s45, s65, 0\n\t"
      "global_load_lds_dwordx4 v68, s[44:45]\n\t"
      "global_load_lds_dwordx4 v69, s[44:45] offset:1024\n\t"
      "s_add_u32 s42, s43, 16384\n\t"
      "s_mov_b32 m0, s42\n\t"
      "s_add_u32 s59, s59, 0x1000\n\t"
      "global_load_lds_dwordx4 v105, s[70:71]\n\t"
      "global_load_lds_dwordx4 v105, s[70:71] offset:1024\n\t"
      "s_mov_b32 m0, s59\n\t"
      "s_add_u32 s44, s44, 0x1000\n\t"
      "s_addc_u32 s45, s45, 0\n\t"
      "global_load_lds_dwordx4 v68, s[44:45]\n\t"
      "s_add_u32 s59, s59, 0x1000\n\t"
      "global_load_lds_dwordx4 v69, s[44:45] offset:1024\n\t"
      "s_mov_b32 m0, s59\n\t"
      "s_add_u32 s44, s44, 0x1000\n\t"
      "s_addc_u32 s45, s45, 0\n\t"
      "global_load_lds_dwordx4 v68, s[44:45]\n\t"
      "s_add_u32 s59, s59, 0x1000\n\t"
      "global_load_lds_dwordx4 v69, s[44:45] offset:1024\n\t"
      "s_add_u32 s42, s40, 0x3000\n\t"
      "s_mov_b32 m0, s42\n\t"
      "s_add_u32 s62, s56, 0x3000\n\t"
      "s_add_u32 s62, s64, s62\n\t"
      "s_addc_u32 s63, s65, 0\n\t"
      "s_mov_b32 exec_hi, 0\n\t"
      "global_load_lds_dwordx4 v68, s[62:63]\n\t"
      "s_mov_b32 exec_hi, -1\n\t"
      "s_add_u32 s59, s59, 14336\n\t"
      "s_add_u32 s48, s74, s57\n\t"
      "s_addc_u32 s49, s75, 0\n\t"
      "s_add_u32 s50, s48, 0x1000\n\t"
      "s_addc_u32 s51, s49, 0\n\t"
      "s_add_u32 s52, s48, 0x2000\n\t"
      "s_addc_u32 s53, s49, 0\n\t"
      "s_add_u32 s76, s48, 25600\n\t"
      "s_addc_u32 s77, s49, 0\n\t"
      "s_add_u32 s78, s76, 0x1000\n\t"
      "s_addc_u32 s79, s77, 0\n\t"
      "s_add_u32 s80, s76, 0x2000\n\t"
      "s_addc_u32 s81, s77, 0\n\t"
      "s_waitcnt vmcnt(11)\n\t"
      "v_lshlrev_b32 v118, 16, v108\n\t"
      "v_and_b32 v119, 0xffff0000, v108\n\t"
      "v_pk_mul_f32 v[118:119], v[118:119], v[116:117]\n\t"
      "v_cvt_pk_bf16_f32 v108, v118, v119\n\t"
      "v_lshlrev_b32 v120, 16, v109\n\t"
      "v_and_b32 v121, 0xffff0000, v109\n\t"
      "v_pk_mul_f32 v[120:121], v[120:121], v[116:117]\n\t"
      "v_cvt_pk_bf16_f32 v109, v120, v121\n\t"
      "v_lshlrev_b32 v118, 16, v110\n\t"
      "v_and_b32 v119, 0xffff0000, v110\n\t"
      "v_pk_mul_f32 v[118:119], v[118:119], v[116:117]\n\t"
      "v_cvt_pk_bf16_f32 v110, v118, v119\n\t"
      "v_lshlrev_b32 v120, 16, v111\n\t"
      "v_and_b32 v121, 0xffff0000, v111\n\t"
      "v_pk_mul_f32 v[120:121], v[120:121], v[116:117]\n\t"
      "v_cvt_pk_bf16_f32 v111, v120, v121\n\t"
      "v_lshlrev_b32 v118, 16, v112\n\t"
      "v_and_b32 v119, 0xffff0000, v112\n\t"
      "v_pk_mul_f32 v[118:119], v[118:119], v[116:117]\n\t"
      "v_cvt_pk_bf16_f32 v112, v118, v119\n\t"
      "v_lshlrev_b32 v120, 16, v113\n\t"
      "v_and_b32 v121, 0xffff0000, v113\n\t"
      "v_pk_mul_f32 v[120:121], v[120:121], v[116:117]\n\t"
      "v_cvt_pk_bf16_f32 v113, v120, v121\n\t"
      "v_lshlrev_b32 v118, 16, v114\n\t"
      "v_and_b32 v119, 0xffff0000, v114\n\t"
      "v_pk_mul_f32 v[118:119], v[118:119], v[116:117]\n\t"
      "v_cvt_pk_bf16_f32 v114, v118, v119\n\t"
      "v_lshlrev_b32 v120, 16, v115\n\t"
      "v_and_b32 v121, 0xffff0000, v115\n\t"
      "v_pk_mul_f32 v[120:121], v[120:121], v[116:117]\n\t"
      "v_cvt_pk_bf16_f32 v115, v120, v121\n\t"
      "ds_write_b128 v105, v[108:111]\n\t"
      "ds_write_b128 v105, v[112:115] offset:1024\n\t"
      "s_add_u32 s42, s43, 24576\n\t"
      "s_mov_b32 m0, s42\n\t"
      "s_add_u32 s42, s43, 23552\n\t"
      "global_load_lds_dwordx4 v106, s[72:73]\n\t"
      "s_mov_b32 m0, s42\n\t"
      "s_nop 0\n\t"
      "global_load_lds_dwordx4 v106, s[72:73] offset:2048\n\t"
      "s_waitcnt vmcnt(7) lgkmcnt(0)\n\t"
      "s_barrier\n\t"
      "ds_read_b64_tr_b16 a[0:1], v1 offset:8192\n\t"
      "ds_read_b64_tr_b16 a[2:3], v1 offset:8448\n\t"
      "ds_read_b64_tr_b16 a[4:5], v1 offset:9216\n\t"
      "ds_read_b64_tr_b16 a[6:7], v1 offset:9472\n\t"
      "ds_read_b64_tr_b16 a[8:9], v1 offset:10240\n\t"
      "ds_read_b64_tr_b16 a[10:11], v1 offset:10496\n\t"
      "ds_read_b64_tr_b16 a[12:13], v1 offset:11264\n\t"
      "v_lshrrev_b32 v104, 2, v2\n\t"
      "ds_read_b64_tr_b16 a[14:15], v1 offset:11520\n\t"
      "v_lshrrev_b32 v105, 4, v2\n\t"
      "ds_read_b128 a[16:19], v3 offset:0\n\t"
      "v_xor_b32 v105, v105, v104\n\t"
      "v_and_b32 v105, 1, v105\n\t"
      "v_add_u32 v105, -1, v105\n\t"
      "ds_read_b128 a[20:23], v152 offset:0\n\t"
      "v_mov_b32 v106, 0x3f803f80\n\t"
      "v_and_b32 v4, v106, v105\n\t"
      "v_accvgpr_write_b32 a24, v4\n\t"
      "ds_read_b128 a[28:31], v224 offset:0\n\t"
      "v_accvgpr_write_b32 a25, v4\n\t"
      "v_accvgpr_write_b32 a26, v4\n\t"
      "v_accvgpr_write_b32 a27, v4\n\t"
      "ds_read_b128 a[32:35], v225 offset:0\n\t"
      "v_mov_b32 v233, 0\n\t"
      "v_mov_b32 v234, 0\n\t"
      "v_lshrrev_b32 v92, 5, v2\n\t"
      "ds_read_b64_tr_b16 a[36:37], v1 offset:0\n\t"
      "v_and_b32 v107, 2, v104\n\t"
      "ds_read_b64_tr_b16 a[38:39], v1 offset:256\n\t"
      "v_and_or_b32 v107, v2, 1, v107\n\t"
      "ds_read_b64_tr_b16 a[40:41], v1 offset:1024\n\t"
      "v_lshrrev_b32 v36, 1, v2\n\t"
      "s_waitcnt lgkmcnt(14)\n\t"
      "ds_read_b64_tr_b16 a[42:43], v1 offset:1280\n\t"
      "v_lshlrev_b32 v108, 1, v2\n\t"
      "s_waitcnt lgkmcnt(14)\n\t"
      "ds_read_b64_tr_b16 a[44:45], v1 offset:2048\n\t"
      "v_and_b32 v109, 1, v36\n\t"
      "s_waitcnt lgkmcnt(14)\n\t"
      "ds_read_b64_tr_b16 a[46:47], v1 offset:2304\n\t"
      "v_add_u32 v109, -1, v109\n\t"
      "s_waitcnt lgkmcnt(14)\n\t"
      "ds_read_b64_tr_b16 a[48:49], v1 offset:3072\n\t"
      "v_and_b32 v110, v105, v109\n\t"
      "s_waitcnt lgkmcnt(14)\n\t"
      "ds_read_b64_tr_b16 a[50:51], v1 offset:3328\n\t"
      "v_xor_b32 v20, v36, v108\n\t"
      "s_waitcnt lgkmcnt(14)\n\t"
      "ds_read_b64_tr_b16 a[52:53], v1 offset:16384\n\t"
      "v_and_b32 v20, 16, v20\n\t"
      "s_waitcnt lgkmcnt(14)\n\t"
      "ds_read_b64_tr_b16 a[54:55], v1 offset:16640\n\t"
      "v_lshl_or_b32 v20, v107, 7, v20\n\t"
      "s_waitcnt lgkmcnt(14)\n\t"
      "ds_read_b64_tr_b16 a[56:57], v1 offset:17408\n\t"
      "v_mov_b32 v21, 0x240\n\t"
      "s_waitcnt lgkmcnt(14)\n\t"
      "v_mfma_f32_32x32x16_bf16 v[4:19], a[0:3], a[16:19], 0\n\t"
      "ds_read_b64_tr_b16 a[58:59], v1 offset:17664\n\t"
      "s_waitcnt lgkmcnt(14)\n\t"
      "ds_read_b64_tr_b16 a[60:61], v1 offset:18432\n\t"
      "s_waitcnt lgkmcnt(14)\n\t"
      "ds_read_b64_tr_b16 a[62:63], v1 offset:18688\n\t"
      "s_waitcnt lgkmcnt(14)\n\t"
      "ds_read_b64_tr_b16 a[64:65], v1 offset:19456\n\t"
      "v_bfi_b32 v20, v110, v20, v21\n\t"
      "s_waitcnt lgkmcnt(14)\n\t"
      "ds_read_b64_tr_b16 a[66:67], v1 offset:19712\n\t"
      "s_nop 0\n\t"
      "v_mfma_f32_32x32x16_bf16 v[4:19], a[4:7], a[20:23], v[4:19]\n\t"
      "v_add_u32 v226, s61, v20\n\t"
      "v_xor_b32 v227, 32, v226\n\t"
      "v_xor_b32 v228, 64, v226\n\t"
      "v_xor_b32 v229, 0x60, v226\n\t"
      "v_mov_b32 v149, 0\n\t"
      "s_waitcnt lgkmcnt(14)\n\t"
      "v_mfma_f32_32x32x16_bf16 v[20:35], a[36:39], a[16:19], 0\n\t"
      "v_mov_b32 v150, 0\n\t"
      "v_mov_b32 v151, 0\n\t"
      "v_mov_b32 v133, 0\n\t"
      "v_mov_b32 v134, 0\n\t"
      "v_mov_b32 v135, 0\n\t"
      "v_mov_b32 v153, 0\n\t"
      "v_mfma_f32_32x32x16_bf16 v[4:19], a[8:11], a[28:31], v[4:19]\n\t"
      "v_mov_b32 v154, 0\n\t"
      "v_mov_b32 v155, 0\n\t"
      "v_lshlrev_b32 v37, 6, v2\n\t"
      "v_and_b32 v37, 0x7c0, v37\n\t"
      "v_lshlrev_b32 v38, 1, v92\n\t"
      "s_waitcnt lgkmcnt(6)\n\t"
      "v_mfma_f32_32x32x16_bf16 v[20:35], a[40:43], a[20:23], v[20:35]\n\t"
      "v_xor_b32 v38, v38, v36\n\t"
      "v_and_b32 v38, 3, v38\n\t"
      "v_lshl_or_b32 v37, v38, 4, v37\n\t"
      "v_add_u32 v235, s41, v37\n\t"
      "v_xor_b32 v236, 16, v235\n\t"
      "v_lshlrev_b32 v36, 4, v2\n\t"
      "v_mfma_f32_32x32x16_bf16 v[4:19], a[12:15], a[32:35], v[4:19]\n\t"
      "v_and_b32 v36, 0x3c0, v36\n\t"
      "v_lshrrev_b32 v37, 3, v2\n\t"
      "v_xor_b32 v37, v37, v2\n\t"
      "v_and_b32 v37, 3, v37\n\t"
      "v_lshl_or_b32 v36, v37, 4, v36\n\t"
      "v_add_u32 v237, s41, v36\n\t"
      "v_mfma_f32_32x32x16_bf16 v[20:35], a[44:47], a[28:31], v[20:35]\n\t"
      "v_mul_u32_u24 v111, 0x110, v107\n\t"
      "v_add_u32 v111, s84, v111\n\t"
      "v_lshlrev_b32 v93, 3, v2\n\t"
      "v_and_b32 v93, 32, v93\n\t"
      "v_and_or_b32 v93, v2, 16, v93\n\t"
      "v_cvt_pk_bf16_f32 v136, v4, v5\n\t"
      "v_mfma_f32_32x32x16_bf16 v[20:35], a[48:51], a[32:35], v[20:35]\n\t"
      "s_waitcnt vmcnt(5)\n\ts_barrier\n\t"
      "v_mfma_f32_32x32x16_bf16 v[36:51], a[16:19], a[52:55], 0\n\t"
      "ds_read_b128 a[68:71], v3 offset:4096\n\t"
      "ds_read_b128 a[72:75], v152 offset:4096\n\t"
      "ds_read_b128 a[76:79], v224 offset:4096\n\t"
      "s_mov_b32 m0, s59\n\ts_add_u32 s44, s44, 0x4400\n\ts_addc_u32 s45, s45, 0\n\tglobal_load_lds_dwordx4 v68, s[44:45]\n\t"
      "s_waitcnt lgkmcnt(3)\n\t"
      "v_mfma_f32_32x32x16_bf16 v[36:51], a[20:23], a[56:59], v[36:51]\n\t"
      "ds_read_b128 a[80:83], v225 offset:4096\n\t"
      "s_add_u32 s59, s59, 0x1000\n\tglobal_load_lds_dwordx4 v69, s[44:45] offset:1024\n\t"
      "v_mfma_f32_32x32x16_bf16 v[36:51], a[28:31], a[60:63], v[36:51]\n\t"
      "s_mov_b32 m0, s59\n\ts_add_u32 s44, s44, 0x1000\n\ts_addc_u32 s45, s45, 0\n\tglobal_load_lds_dwordx4 v68, s[44:45]\n\t"
      "v_mfma_f32_32x32x16_bf16 v[36:51], a[32:35], a[64:67], v[36:51]\n\t"
      "s_add_u32 s59, s59, 0x1000\n\tglobal_load_lds_dwordx4 v69, s[44:45] offset:1024\n\t"
      "s_waitcnt lgkmcnt(1)\n\t"
      "v_mfma_f32_32x32x16_bf16 v[52:67], a[0:3], a[68:71], 0\n\t"
      "s_mov_b32 m0, s59\n\ts_add_u32 s44, s44, 0x1000\n\ts_addc_u32 s45, s45, 0\n\tglobal_load_lds_dwordx4 v68, s[44:45]\n\t"
      "v_mfma_f32_32x32x16_bf16 v[52:67], a[4:7], a[72:75], v[52:67]\n\t"
      "s_add_u32 s59, s59, 0x1000\n\tglobal_load_lds_dwordx4 v69, s[44:45] offset:1024\n\t"
      "v_mfma_f32_32x32x16_bf16 v[52:67], a[8:11], a[76:79], v[52:67]\n\t"
      "s_add_u32 s42, s40, 0x9800\n\ts_mov_b32 m0, s42\n\ts_add_u32 s62, s56, 0x9400\n\ts_add_u32 s62, s64, s62\n\ts_addc_u32 s63, s65, 0\n\ts_mov_b32 exec_hi, 0\n\tglobal_load_lds_dwordx4 v68, s[62:63]\n\ts_mov_b32 exec_hi, -1\n\t"
      "s_waitcnt lgkmcnt(0)\n\t"
      "v_mfma_f32_32x32x16_bf16 v[52:67], a[12:15], a[80:83], v[52:67]\n\t"
      "v_cvt_pk_bf16_f32 v137, v6, v7\n\t"
      "v_cvt_pk_bf16_f32 v138, v8, v9\n\t"
      "v_cvt_pk_bf16_f32 v139, v10, v11\n\t"
      "v_cvt_pk_bf16_f32 v140, v12, v13\n\t"
      "v_cvt_pk_bf16_f32 v141, v14, v15\n\t"
      "v_cvt_pk_bf16_f32 v142, v16, v17\n\t"
      "v_mfma_f32_32x32x16_bf16 v[68:83], a[36:39], a[68:71], 0\n\t"
      "v_cvt_pk_bf16_f32 v143, v18, v19\n\t"
      "v_cvt_pk_bf16_f32 v84, v20, v21\n\t"
      "v_cvt_pk_bf16_f32 v85, v22, v23\n\t"
      "v_cvt_pk_bf16_f32 v86, v24, v25\n\t"
      "v_cvt_pk_bf16_f32 v87, v26, v27\n\t"
      "v_cvt_pk_bf16_f32 v88, v28, v29\n\t"
      "v_mfma_f32_32x32x16_bf16 v[68:83], a[40:43], a[72:75], v[68:83]\n\t"
      "v_cvt_pk_bf16_f32 v89, v30, v31\n\t"
      "v_cvt_pk_bf16_f32 v90, v32, v33\n\t"
      "v_cvt_pk_bf16_f32 v91, v34, v35\n\t"
      "v_cvt_pk_bf16_f32 v144, v36, v37\n\t"
      "v_cvt_pk_bf16_f32 v145, v38, v39\n\t"
      "v_cvt_pk_bf16_f32 v146, v40, v41\n\t"
      "v_mfma_f32_32x32x16_bf16 v[4:19], v[136:139], v[84:87], 0\n\t"
      "v_cvt_pk_bf16_f32 v147, v42, v43\n\t"
      "v_cvt_pk_bf16_f32 v156, v44, v45\n\t"
      "v_cvt_pk_bf16_f32 v157, v46, v47\n\t"
      "v_cvt_pk_bf16_f32 v158, v48, v49\n\t"
      "v_cvt_pk_bf16_f32 v159, v50, v51\n\t"
      "v_cvt_pk_bf16_f32 v160, v52, v53\n\t"
      "v_mfma_f32_32x32x16_bf16 v[4:19], v[140:143], v[88:91], v[4:19]\n\t"
      "v_cvt_pk_bf16_f32 v161, v54, v55\n\t"
      "v_cvt_pk_bf16_f32 v162, v56, v57\n\t"
      "v_cvt_pk_bf16_f32 v163, v58, v59\n\t"
      "v_cvt_pk_bf16_f32 v164, v60, v61\n\t"
      "v_cvt_pk_bf16_f32 v165, v62, v63\n\t"
      "v_cvt_pk_bf16_f32 v166, v64, v65\n\t"
      "v_mfma_f32_32x32x16_bf16 v[68:83], a[44:47], a[76:79], v[68:83]\n\t"
      "v_cvt_pk_bf16_f32 v167, v66, v67\n\t"
      "v_and_or_b32 v93, v104, 8, v93\n\t"
      "v_add_u32 v238, v111, v93\n\t"
      "v_bfi_b32 v238, v109, v238, s86\n\t"
      "v_lshl_add_u32 v111, v92, 4, v111\n\t"
      "v_exp_f32 v4, v4\n\t"
      "v_mfma_f32_32x32x16_bf16 v[68:83], a[48:51], a[80:83], v[68:83]\n\t"
      "v_exp_f32 v5, v5\n\t"
      "v_exp_f32 v6, v6\n\t"
      "v_exp_f32 v7, v7\n\t"
      "v_mfma_f32_32x32x16_bf16 v[20:35], v[160:163], v[84:87], 0\n\t"
      "v_exp_f32 v8, v8\n\t"
      "v_exp_f32 v9, v9\n\t"
      "v_exp_f32 v10, v10\n\t"
      "v_mfma_f32_32x32x16_bf16 v[20:35], v[164:167], v[88:91], v[20:35]\n\t"
      "v_exp_f32 v11, v11\n\t"
      "v_cvt_pk_bf16_f32 v4, v4, v5\n\t"
      "v_cvt_pk_bf16_f32 v5, v6, v7\n\t"
      "v_cvt_pk_bf16_f32 v6, v8, v9\n\t"
      "v_cvt_pk_bf16_f32 v7, v10, v11\n\t"
      "v_mfma_f32_32x32x16_bf16 v[36:51], a[68:71], a[52:55], 0\n\t"
      "v_exp_f32 v12, v12\n\t"
      "v_exp_f32 v13, v13\n\t"
      "v_exp_f32 v14, v14\n\t"
      "v_mfma_f32_32x32x16_bf16 v[52:67], v[144:147], v[4:7], 0\n\t"
      "v_exp_f32 v15, v15\n\t"
      "v_exp_f32 v16, v16\n\t"
      "v_exp_f32 v17, v17\n\t"
      "v_mfma_f32_16x16x32_bf16 v[92:95], a[24:27], v[4:7], 0\n\t"
      "v_exp_f32 v18, v18\n\t"
      "v_mfma_f32_32x32x16_bf16 v[36:51], a[72:75], a[56:59], v[36:51]\n\t"
      "v_exp_f32 v19, v19\n\t"
      "v_cvt_pk_bf16_f32 v12, v12, v13\n\t"
      "v_cvt_pk_bf16_f32 v13, v14, v15\n\t"
      "v_cvt_pk_bf16_f32 v14, v16, v17\n\t"
      "v_cvt_pk_bf16_f32 v15, v18, v19\n\t"
      "v_mfma_f32_32x32x16_bf16 v[36:51], a[76:79], a[60:63], v[36:51]\n\t"
      "v_cvt_pk_bf16_f32 v96, v68, v69\n\t"
      "v_cvt_pk_bf16_f32 v97, v70, v71\n\t"
      "v_cvt_pk_bf16_f32 v98, v72, v73\n\t"
      "v_cvt_pk_bf16_f32 v99, v74, v75\n\t"
      "v_cvt_pk_bf16_f32 v100, v76, v77\n\t"
      "v_cvt_pk_bf16_f32 v101, v78, v79\n\t"
      "v_mfma_f32_32x32x16_bf16 v[52:67], v[156:159], v[12:15], v[52:67]\n\t"
      "v_cvt_pk_bf16_f32 v102, v80, v81\n\t"
      "v_cvt_pk_bf16_f32 v103, v82, v83\n\t"
      "v_exp_f32 v20, v20\n\t"
      "v_exp_f32 v21, v21\n\t"
      "v_mfma_f32_16x16x32_bf16 v[92:95], a[24:27], v[12:15], v[92:95]\n\t"
      "v_exp_f32 v22, v22\n\t"
      "v_mfma_f32_32x32x16_bf16 v[36:51], a[80:83], a[64:67], v[36:51]\n\t"
      "v_exp_f32 v23, v23\n\t"
      "v_exp_f32 v24, v24\n\t"
      "v_exp_f32 v25, v25\n\t"
      "v_mfma_f32_32x32x16_bf16 v[4:19], v[136:139], v[96:99], 0\n\t"
      "v_exp_f32 v26, v26\n\t"
      "v_exp_f32 v27, v27\n\t"
      "v_cvt_pk_bf16_f32 v20, v20, v21\n\t"
      "v_cvt_pk_bf16_f32 v21, v22, v23\n\t"
      "v_mfma_f32_32x32x16_bf16 v[4:19], v[140:143], v[100:103], v[4:19]\n\t"
      "v_cvt_pk_bf16_f32 v22, v24, v25\n\t"
      "v_cvt_pk_bf16_f32 v23, v26, v27\n\t"
      "v_exp_f32 v28, v28\n\t"
      "v_cvt_pk_bf16_f32 v168, v36, v37\n\t"
      "v_cvt_pk_bf16_f32 v169, v38, v39\n\t"
      "v_mfma_f32_16x16x32_bf16 v[92:95], a[24:27], v[20:23], v[92:95]\n\t"
      "v_cvt_pk_bf16_f32 v170, v40, v41\n\t"
      "v_cvt_pk_bf16_f32 v171, v42, v43\n\t"
      "v_mfma_f32_32x32x16_bf16 v[68:83], v[160:163], v[96:99], 0\n\t"
      "v_cvt_pk_bf16_f32 v172, v44, v45\n\t"
      "v_cvt_pk_bf16_f32 v173, v46, v47\n\t"
      "v_cvt_pk_bf16_f32 v174, v48, v49\n\t"
      "v_cvt_pk_bf16_f32 v175, v50, v51\n\t"
      "v_exp_f32 v29, v29\n\t"
      "v_mfma_f32_32x32x16_bf16 v[52:67], v[168:171], v[20:23], v[52:67]\n\t"
      "v_exp_f32 v30, v30\n\t"
      "v_exp_f32 v31, v31\n\t"
      "v_exp_f32 v32, v32\n\t"
      "v_mfma_f32_32x32x16_bf16 v[68:83], v[164:167], v[100:103], v[68:83]\n\t"
      "v_exp_f32 v33, v33\n\t"
      "v_exp_f32 v34, v34\n\t"
      "v_exp_f32 v35, v35\n\t"
      "v_cvt_pk_bf16_f32 v28, v28, v29\n\t"
      "v_cvt_pk_bf16_f32 v29, v30, v31\n\t"
      "v_cvt_pk_bf16_f32 v30, v32, v33\n\t"
      "v_cvt_pk_bf16_f32 v31, v34, v35\n\t"
      "v_exp_f32 v4, v4\n\t"
      "v_exp_f32 v5, v5\n\t"
      "v_mfma_f32_32x32x16_bf16 v[52:67], v[172:175], v[28:31], v[52:67]\n\t"
      "v_exp_f32 v6, v6\n\t"
      "v_exp_f32 v7, v7\n\t"
      "v_exp_f32 v8, v8\n\t"
      "v_mfma_f32_16x16x32_bf16 v[92:95], a[24:27], v[28:31], v[92:95]\n\t"
      "v_exp_f32 v9, v9\n\t"
      "v_exp_f32 v10, v10\n\t"
      "v_exp_f32 v11, v11\n\t"
      "v_cvt_pk_bf16_f32 v4, v4, v5\n\t"
      "v_cvt_pk_bf16_f32 v5, v6, v7\n\t"
      "v_cvt_pk_bf16_f32 v6, v8, v9\n\t"
      "v_cvt_pk_bf16_f32 v7, v10, v11\n\t"
      "v_exp_f32 v12, v12\n\t"
      "v_exp_f32 v13, v13\n\t"
      "v_mfma_f32_32x32x16_bf16 v[20:35], v[144:147], v[4:7], 0\n\t"
      "v_exp_f32 v14, v14\n\t"
      "v_exp_f32 v15, v15\n\t"
      "v_exp_f32 v16, v16\n\t"
      "v_mfma_f32_16x16x32_bf16 v[176:179], a[24:27], v[4:7], 0\n\t"
      "v_exp_f32 v17, v17\n\t"
      "v_exp_f32 v18, v18\n\t"
      "v_exp_f32 v19, v19\n\t"
      "v_cvt_pk_bf16_f32 v12, v12, v13\n\t"
      "v_cvt_pk_bf16_f32 v13, v14, v15\n\t"
      "v_cvt_pk_bf16_f32 v14, v16, v17\n\t"
      "v_cvt_pk_bf16_f32 v15, v18, v19\n\t"
      "v_exp_f32 v68, v68\n\t"
      "v_exp_f32 v69, v69\n\t"
      "v_mfma_f32_32x32x16_bf16 v[20:35], v[156:159], v[12:15], v[20:35]\n\t"
      "v_exp_f32 v70, v70\n\t"
      "v_exp_f32 v71, v71\n\t"
      "v_exp_f32 v72, v72\n\t"
      "v_mfma_f32_16x16x32_bf16 v[176:179], a[24:27], v[12:15], v[176:179]\n\t"
      "s_waitcnt vmcnt(7)\n\ts_barrier\n\t"
      "ds_read_b128 a[84:87], v3 offset:8192\n\t"
      "ds_read_b128 a[88:91], v152 offset:8192\n\t"
      "v_exp_f32 v73, v73\n\t"
      "ds_read_b128 a[92:95], v224 offset:8192\n\t"
      "v_exp_f32 v74, v74\n\t"
      "ds_read_b128 a[96:99], v225 offset:8192\n\t"
      "v_exp_f32 v75, v75\n\t"
      "v_cvt_pk_bf16_f32 v68, v68, v69\n\t"
      "ds_read_b128 a[100:103], v226 offset:0\n\t"
      "v_cvt_pk_bf16_f32 v69, v70, v71\n\t"
      "v_cvt_pk_bf16_f32 v70, v72, v73\n\t"
      "ds_read_b128 a[104:107], v227 offset:0\n\t"
      "v_cvt_pk_bf16_f32 v71, v74, v75\n\t"
      "v_exp_f32 v76, v76\n\t"
      "ds_read_b128 a[108:111], v228 offset:0\n\t"
      "v_mfma_f32_32x32x16_bf16 v[20:35], v[168:171], v[68:71], v[20:35]\n\t"
      "v_exp_f32 v77, v77\n\t"
      "ds_read_b128 a[112:115], v229 offset:0\n\t"
      "v_exp_f32 v78, v78\n\t"
      "ds_read_b64_tr_b16 a[116:117], v1 offset:24576\n\t"
      "v_mfma_f32_16x16x32_bf16 v[176:179], a[24:27], v[68:71], v[176:179]\n\t"
      "ds_read_b64_tr_b16 a[118:119], v1 offset:24832\n\t"
      "ds_read_b64_tr_b16 a[120:121], v1 offset:25600\n\t"
      "v_cvt_pk_bf16_f32 v76, v76, v77\n\t"
      "s_waitcnt lgkmcnt(10)\n\t"
      "v_mfma_f32_32x32x16_bf16 v[4:19], a[0:3], a[84:87], 0\n\t"
      "ds_read_b64_tr_b16 a[122:123], v1 offset:25856\n\t"
      "ds_read_b64_tr_b16 a[124:125], v1 offset:26624\n\t"
      "ds_read_b64_tr_b16 a[126:127], v1 offset:26880\n\t"
      "ds_read_b64_tr_b16 a[128:129], v1 offset:27648\n\t"
      "v_exp_f32 v79, v79\n\t"
      "ds_read_b64_tr_b16 a[130:131], v1 offset:27904\n\t"
      "s_waitcnt lgkmcnt(13)\n\t"
      "v_mfma_f32_32x32x16_bf16 v[4:19], a[4:7], a[88:91], v[4:19]\n\t"
      "v_exp_f32 v80, v80\n\t"
      "v_exp_f32 v81, v81\n\t"
      "v_cvt_pk_bf16_f32 v77, v78, v79\n\t"
      "s_waitcnt lgkmcnt(11)\n\t"
      "v_mfma_f32_32x32x16_bf16 v[36:51], a[0:3], a[100:103], 0\n\t"
      "v_exp_f32 v82, v82\n\t"
      "v_exp_f32 v83, v83\n\t"
      "v_cvt_pk_bf16_f32 v78, v80, v81\n\t"
      "v_cvt_pk_bf16_f32 v79, v82, v83\n\t"
      "v_mfma_f32_32x32x16_bf16 v[4:19], a[8:11], a[92:95], v[4:19]\n\t"
      "v_bfi_b32 v1, v105, 0, 8\n\t"
      "v_add_u32 v239, v111, v1\n\t"
      "v_bfi_b32 v239, v109, v239, s86\n\t"
      "v_bfi_b32 v1, v110, v111, s85\n\t"
      "v_lshlrev_b32 v68, 2, v2\n\t"
      "s_waitcnt lgkmcnt(5)\n\t"
      "v_mfma_f32_32x32x16_bf16 v[36:51], a[4:7], a[104:107], v[36:51]\n\t"
      "v_and_b32 v2, 32, v108\n\t"
      "v_and_or_b32 v2, v68, 16, v2\n\t"
      "v_and_or_b32 v2, v104, 8, v2\n\t"
      "v_lshl_or_b32 v2, v107, 7, v2\n\t"
      "v_add_u32 v240, 0x3000, v2\n\t"
      "v_accvgpr_write_b32 a132, v106\n\t"
      "v_mfma_f32_32x32x16_bf16 v[4:19], a[12:15], a[96:99], v[4:19]\n\t"
      "v_accvgpr_write_b32 a133, v106\n\t"
      "v_accvgpr_write_b32 a134, v106\n\t"
      "v_accvgpr_write_b32 a135, v106\n\t"
      "v_mov_b32 v2, 0xf149f2ca\n\t"
      "v_bfi_b32 v180, v105, 0, v2\n\t"
      "v_mov_b32 v181, v180\n\t"
      "v_mfma_f32_32x32x16_bf16 v[20:35], v[172:175], v[76:79], v[20:35]\n\t"
      "s_nop 4\n\t"
      "v_cvt_pk_bf16_f32 v184, v4, v5\n\t"
      "v_mfma_f32_16x16x32_bf16 v[176:179], a[24:27], v[76:79], v[176:179]\n\t"
      "v_cvt_pk_bf16_f32 v185, v6, v7\n\t"
      "v_cvt_pk_bf16_f32 v186, v8, v9\n\t"
      "v_mfma_f32_32x32x16_bf16 v[36:51], a[8:11], a[108:111], v[36:51]\n\t"
      "v_cvt_pk_bf16_f32 v187, v10, v11\n\t"
      "v_cvt_pk_bf16_f32 v188, v12, v13\n\t"
      "v_cvt_pk_bf16_f32 v189, v14, v15\n\t"
      "v_cvt_pk_bf16_f32 v190, v16, v17\n\t"
      "v_cvt_pk_bf16_f32 v191, v18, v19\n\t"
      "v_mfma_f32_32x32x16_bf16 v[4:19], v[184:187], v[84:87], 0\n\t"
      "s_nop 5\n\t"
      "v_mfma_f32_32x32x16_bf16 v[4:19], v[188:191], v[88:91], v[4:19]\n\t"
      "s_nop 5\n\t"
      "v_mfma_f32_32x32x16_bf16 v[36:51], a[12:15], a[112:115], v[36:51]\n\t"
      "s_nop 4\n\t"
      "v_exp_f32 v4, v4\n\t"
      "v_mfma_f32_32x32x16_bf16 v[68:83], a[84:87], a[52:55], 0\n\t"
      "v_exp_f32 v5, v5\n\t"
      "v_exp_f32 v6, v6\n\t"
      "v_exp_f32 v7, v7\n\t"
      "v_mfma_f32_32x32x16_bf16 v[68:83], a[88:91], a[56:59], v[68:83]\n\t"
      "v_exp_f32 v8, v8\n\t"
      "v_cvt_pk_bf16_f32 v192, v36, v37\n\t"
      "v_cvt_pk_bf16_f32 v193, v38, v39\n\t"
      "v_cvt_pk_bf16_f32 v194, v40, v41\n\t"
      "v_cvt_pk_bf16_f32 v195, v42, v43\n\t"
      "v_mfma_f32_32x32x16_bf16 v[68:83], a[92:95], a[60:63], v[68:83]\n\t"
      "v_cvt_pk_bf16_f32 v196, v44, v45\n\t"
      "v_cvt_pk_bf16_f32 v197, v46, v47\n\t"
      "v_cvt_pk_bf16_f32 v198, v48, v49\n\t"
      "v_cvt_pk_bf16_f32 v199, v50, v51\n\t"
      "v_exp_f32 v9, v9\n\t"
      "v_mfma_f32_32x32x16_bf16 v[68:83], a[96:99], a[64:67], v[68:83]\n\t"
      "v_exp_f32 v10, v10\n\t"
      "v_exp_f32 v11, v11\n\t"
      "v_cvt_pk_bf16_f32 v4, v4, v5\n\t"
      "v_cvt_pk_bf16_f32 v5, v6, v7\n\t"
      "v_mfma_f32_32x32x16_bf16 v[36:51], v[184:187], v[96:99], 0\n\t"
      "v_cvt_pk_bf16_f32 v6, v8, v9\n\t"
      "v_cvt_pk_bf16_f32 v7, v10, v11\n\t"
      "v_exp_f32 v12, v12\n\t"
      "v_exp_f32 v13, v13\n\t"
      "v_mfma_f32_32x32x16_bf16 v[36:51], v[188:191], v[100:103], v[36:51]\n\t"
      "v_exp_f32 v14, v14\n\t"
      "v_exp_f32 v15, v15\n\t"
      "v_cvt_pk_bf16_f32 v200, v68, v69\n\t"
      "v_cvt_pk_bf16_f32 v201, v70, v71\n\t"
      "v_mfma_f32_16x16x32_bf16 v[8:11], a[100:103], a[52:55], 0\n\t"
      "v_cvt_pk_bf16_f32 v202, v72, v73\n\t"
      "v_cvt_pk_bf16_f32 v203, v74, v75\n\t"
      "v_mfma_f32_16x16x32_bf16 v[8:11], a[104:107], a[56:59], v[8:11]\n\t"
      "v_cvt_pk_bf16_f32 v204, v76, v77\n\t"
      "v_cvt_pk_bf16_f32 v205, v78, v79\n\t"
      "v_mfma_f32_16x16x32_bf16 v[8:11], a[108:111], a[60:63], v[8:11]\n\t"
      "v_cvt_pk_bf16_f32 v206, v80, v81\n\t"
      "v_cvt_pk_bf16_f32 v207, v82, v83\n\t"
      "v_mfma_f32_16x16x32_bf16 v[8:11], a[112:115], a[64:67], v[8:11]\n\t"
      "v_exp_f32 v16, v16\n\t"
      "v_mfma_f32_16x16x32_bf16 v[68:71], v[192:195], v[84:87], 0\n\t"
      "v_exp_f32 v17, v17\n\t"
      "v_mfma_f32_16x16x32_bf16 v[68:71], v[196:199], v[88:91], v[68:71]\n\t"
      "v_exp_f32 v18, v18\n\t"
      "v_mfma_f32_16x16x32_bf16 v[84:87], v[192:195], v[96:99], 0\n\t"
      "v_exp_f32 v19, v19\n\t"
      "v_mfma_f32_16x16x32_bf16 v[84:87], v[196:199], v[100:103], v[84:87]\n\t"
      "v_cvt_pk_bf16_f32 v132, v8, v9\n\t"
      "v_cvt_pk_bf16_f32 v12, v12, v13\n\t"
      "v_mfma_f32_32x32x16_bf16 v[52:67], v[200:203], v[4:7], v[52:67]\n\t"
      "v_cvt_pk_bf16_f32 v13, v14, v15\n\t"
      "v_cvt_pk_bf16_f32 v14, v16, v17\n\t"
      "v_cvt_pk_bf16_f32 v15, v18, v19\n\t"
      "v_exp_f32 v68, v68\n\t"
      "v_exp_f32 v69, v69\n\t"
      "v_mfma_f32_16x16x32_bf16 v[92:95], a[24:27], v[4:7], v[92:95]\n\t"
      "v_cvt_pk_bf16_f32 v148, v68, v69\n\t"
      "v_exp_f32 v36, v36\n\t"
      "v_mfma_f32_32x32x16_bf16 v[52:67], v[204:207], v[12:15], v[52:67]\n\t"
      "v_exp_f32 v37, v37\n\t"
      "v_exp_f32 v38, v38\n\t"
      "v_exp_f32 v39, v39\n\t"
      "v_mfma_f32_16x16x32_bf16 v[92:95], a[24:27], v[12:15], v[92:95]\n\t"
      "v_exp_f32 v40, v40\n\t"
      "v_mfma_f32_32x32x16_bf16 v[4:19], a[36:39], a[84:87], 0\n\t"
      "v_exp_f32 v41, v41\n\t"
      "v_exp_f32 v42, v42\n\t"
      "v_exp_f32 v43, v43\n\t"
      "v_mfma_f32_32x32x16_bf16 v[4:19], a[40:43], a[88:91], v[4:19]\n\t"
      "v_cvt_pk_bf16_f32 v36, v36, v37\n\t"
      "v_cvt_pk_bf16_f32 v37, v38, v39\n\t"
      "v_cvt_pk_bf16_f32 v38, v40, v41\n\t"
      "v_cvt_pk_bf16_f32 v39, v42, v43\n\t"
      "v_exp_f32 v44, v44\n\t"
      "v_mfma_f32_32x32x16_bf16 v[4:19], a[44:47], a[92:95], v[4:19]\n\t"
      "v_exp_f32 v45, v45\n\t"
      "v_exp_f32 v46, v46\n\t"
      "v_exp_f32 v47, v47\n\t"
      "v_mfma_f32_32x32x16_bf16 v[4:19], a[48:51], a[96:99], v[4:19]\n\t"
      "v_exp_f32 v48, v48\n\t"
      "v_exp_f32 v49, v49\n\t"
      "v_exp_f32 v50, v50\n\t"
      "v_mfma_f32_32x32x16_bf16 v[68:83], a[116:119], a[16:19], 0\n\t"
      "v_exp_f32 v51, v51\n\t"
      "v_cvt_pk_bf16_f32 v44, v44, v45\n\t"
      "v_cvt_pk_bf16_f32 v45, v46, v47\n\t"
      "v_cvt_pk_bf16_f32 v46, v48, v49\n\t"
      "v_cvt_pk_bf16_f32 v47, v50, v51\n\t"
      "v_mfma_f32_32x32x16_bf16 v[52:67], v[132:135], v[148:151], v[52:67]\n\t"
      "s_waitcnt lgkmcnt(0)\n\t"
      "v_exp_f32 v84, v84\n\t"
      "v_exp_f32 v85, v85\n\t"
      "v_cvt_pk_bf16_f32 v208, v4, v5\n\t"
      "v_mfma_f32_32x32x16_bf16 v[68:83], a[120:123], a[20:23], v[68:83]\n\t"
      "v_cvt_pk_bf16_f32 v209, v6, v7\n\t"
      "v_cvt_pk_bf16_f32 v210, v8, v9\n\t"
      "v_cvt_pk_bf16_f32 v211, v10, v11\n\t"
      "v_cvt_pk_bf16_f32 v212, v12, v13\n\t"
      "v_cvt_pk_bf16_f32 v213, v14, v15\n\t"
      "v_cvt_pk_bf16_f32 v214, v16, v17\n\t"
      "v_mfma_f32_16x16x32_bf16 v[92:95], a[24:27], v[148:151], v[92:95]\n\t"
      "v_cvt_pk_bf16_f32 v215, v18, v19\n\t"
      "v_cvt_pk_bf16_f32 v148, v84, v85\n\t"
      "v_mfma_f32_32x32x16_bf16 v[20:35], v[200:203], v[36:39], v[20:35]\n\t"
      "s_nop 4\n\t"
      "v_subrev_u32 v2, 0x2b800000, v92\n\t"
      "v_mfma_f32_16x16x32_bf16 v[176:179], a[24:27], v[36:39], v[176:179]\n\t"
      "v_rcp_f32 v182, v92\n\t"
      "v_mfma_f32_32x32x16_bf16 v[20:35], v[204:207], v[44:47], v[20:35]\n\t"
      "v_max_u32 v233, v233, v2\n\t"
      "s_nop 4\n\t"
      "v_mfma_f32_16x16x32_bf16 v[176:179], a[24:27], v[44:47], v[176:179]\n\t"
      "s_waitcnt vmcnt(0)\n\ts_barrier\n\t"
      "v_mfma_f32_32x32x16_bf16 v[20:35], v[132:135], v[148:151], v[20:35]\n\t"
      "ds_read_b128 a[16:19], v3 offset:26624\n\t"
      "ds_read_b128 a[20:23], v152 offset:26624\n\t"
      "ds_read_b128 a[136:139], v224 offset:26624\n\t"
      "s_nop 2\n\t"
      "v_mfma_f32_16x16x32_bf16 v[176:179], a[24:27], v[148:151], v[176:179]\n\t"
      "ds_read_b128 a[140:143], v225 offset:26624\n\t"
      "s_nop 0\n\t"
      "v_mfma_f32_32x32x16_bf16 v[4:19], v[136:139], v[208:211], 0\n\t"
      "ds_read_b128 a[144:147], v3 offset:30720\n\t"
      "s_nop 0\n\t"
      "ds_read_b128 a[148:151], v152 offset:30720\n\t"
      "s_nop 2\n\t"
      "v_mfma_f32_32x32x16_bf16 v[4:19], v[140:143], v[212:215], v[4:19]\n\t"
      "ds_read_b128 a[152:155], v224 offset:30720\n\t"
      "s_nop 0\n\t"
      "ds_read_b128 a[156:159], v225 offset:30720\n\t"
      "s_nop 2\n\t"
      "v_mfma_f32_32x32x16_bf16 v[36:51], a[36:39], a[100:103], 0\n\t"
      "s_nop 4\n\t"
      "v_exp_f32 v4, v4\n\t"
      "v_mfma_f32_32x32x16_bf16 v[36:51], a[40:43], a[104:107], v[36:51]\n\t"
      "v_exp_f32 v5, v5\n\t"
      "v_exp_f32 v6, v6\n\t"
      "v_exp_f32 v7, v7\n\t"
      "v_mfma_f32_32x32x16_bf16 v[36:51], a[44:47], a[108:111], v[36:51]\n\t"
      "v_exp_f32 v8, v8\n\t"
      "v_exp_f32 v9, v9\n\t"
      "v_exp_f32 v10, v10\n\t"
      "v_mfma_f32_32x32x16_bf16 v[36:51], a[48:51], a[112:115], v[36:51]\n\t"
      "v_exp_f32 v11, v11\n\t"
      "v_cvt_pk_bf16_f32 v4, v4, v5\n\t"
      "v_cvt_pk_bf16_f32 v5, v6, v7\n\t"
      "v_cvt_pk_bf16_f32 v6, v8, v9\n\t"
      "v_cvt_pk_bf16_f32 v7, v10, v11\n\t"
      "v_mfma_f32_32x32x16_bf16 v[68:83], a[124:127], a[28:31], v[68:83]\n\t"
      "v_exp_f32 v12, v12\n\t"
      "v_exp_f32 v13, v13\n\t"
      "v_exp_f32 v14, v14\n\t"
      "v_mfma_f32_32x32x16_bf16 v[68:83], a[128:131], a[32:35], v[68:83]\n\t"
      "v_exp_f32 v15, v15\n\t"
      "v_exp_f32 v16, v16\n\t"
      "v_cvt_pk_bf16_f32 v8, v36, v37\n\t"
      "v_cvt_pk_bf16_f32 v9, v38, v39\n\t"
      "v_mfma_f32_32x32x16_bf16 v[84:99], a[116:119], a[68:71], 0\n\t"
      "s_waitcnt lgkmcnt(0)\n\t"
      "v_cvt_pk_bf16_f32 v10, v40, v41\n\t"
      "v_cvt_pk_bf16_f32 v11, v42, v43\n\t"
      "v_cvt_pk_bf16_f32 v36, v44, v45\n\t"
      "v_cvt_pk_bf16_f32 v37, v46, v47\n\t"
      "v_cvt_pk_bf16_f32 v38, v48, v49\n\t"
      "v_mfma_f32_32x32x16_bf16 v[100:115], a[0:3], a[16:19], 0\n\t"
      "v_fmac_f32 v68, v52, v182\n\t"
      "v_fmac_f32 v69, v53, v182\n\t"
      "v_cvt_pk_bf16_f32 v68, v68, v69\n\t"
      "v_pk_max_i16 v68, v68, 0\n\t"
      "v_fmac_f32 v70, v54, v182\n\t"
      "v_fmac_f32 v71, v55, v182\n\t"
      "v_mfma_f32_32x32x16_bf16 v[84:99], a[120:123], a[72:75], v[84:99]\n\t"
      "v_cvt_pk_bf16_f32 v69, v70, v71\n\t"
      "v_pk_max_i16 v69, v69, 0\n\t"
      "v_fmac_f32 v72, v56, v182\n\t"
      "v_fmac_f32 v73, v57, v182\n\t"
      "v_cvt_pk_bf16_f32 v70, v72, v73\n\t"
      "v_pk_max_i16 v70, v70, 0\n\t"
      "v_mfma_f32_16x16x32_bf16 v[216:219], a[116:119], a[100:103], 0\n\t"
      "v_fmac_f32 v74, v58, v182\n\t"
      "v_fmac_f32 v75, v59, v182\n\t"
      "v_mfma_f32_16x16x32_bf16 v[216:219], a[120:123], a[104:107], v[216:219]\n\t"
      "v_cvt_pk_bf16_f32 v71, v74, v75\n\t"
      "v_pk_max_i16 v71, v71, 0\n\t"
      "v_mfma_f32_16x16x32_bf16 v[216:219], a[124:127], a[108:111], v[216:219]\n\t"
      "v_fmac_f32 v76, v60, v182\n\t"
      "v_fmac_f32 v77, v61, v182\n\t"
      "v_mfma_f32_16x16x32_bf16 v[216:219], a[128:131], a[112:115], v[216:219]\n\t"
      "v_cvt_pk_bf16_f32 v72, v76, v77\n\t"
      "v_pk_max_i16 v72, v72, 0\n\t"
      "v_mfma_f32_32x32x16_bf16 v[116:131], v[160:163], v[208:211], 0\n\t"
      "v_fmac_f32 v78, v62, v182\n\t"
      "v_fmac_f32 v79, v63, v182\n\t"
      "v_cvt_pk_bf16_f32 v73, v78, v79\n\t"
      "v_pk_max_i16 v73, v73, 0\n\t"
      "v_fmac_f32 v80, v64, v182\n\t"
      "v_fmac_f32 v81, v65, v182\n\t"
      "v_mfma_f32_32x32x16_bf16 v[116:131], v[164:167], v[212:215], v[116:131]\n\t"
      "v_cvt_pk_bf16_f32 v74, v80, v81\n\t"
      "v_pk_max_i16 v74, v74, 0\n\t"
      "v_fmac_f32 v82, v66, v182\n\t"
      "v_fmac_f32 v83, v67, v182\n\t"
      "v_cvt_pk_bf16_f32 v75, v82, v83\n\t"
      "v_pk_max_i16 v75, v75, 0\n\t"
      "v_mfma_f32_16x16x32_bf16 v[52:55], v[136:139], v[8:11], 0\n\t"
      "ds_write_b128 v235, v[68:71]\n\t"
      "v_mfma_f32_16x16x32_bf16 v[56:59], v[160:163], v[8:11], 0\n\t"
      "ds_write_b128 v236, v[72:75]\n\t"
      "v_mfma_f32_16x16x32_bf16 v[68:71], v[184:187], v[8:11], 0\n\t"
      "v_cvt_pk_bf16_f32 v39, v50, v51\n\t"
      "v_cvt_pk_bf16_f32 v12, v12, v13\n\t"
      "v_mfma_f32_16x16x32_bf16 v[72:75], v[192:195], v[8:11], v[180:183]\n\t"
      "ds_read_b128 a[28:31], v237\n\t"
      "v_cvt_pk_bf16_f32 v13, v14, v15\n\t"
      "v_mfma_f32_16x16x32_bf16 v[52:55], v[140:143], v[36:39], v[52:55]\n\t"
      "ds_read_b128 a[32:35], v237 offset:1024\n\t"
      "v_exp_f32 v17, v17\n\t"
      "v_mfma_f32_16x16x32_bf16 v[56:59], v[164:167], v[36:39], v[56:59]\n\t"
      "v_exp_f32 v18, v18\n\t"
      "v_mfma_f32_16x16x32_bf16 v[68:71], v[188:191], v[36:39], v[68:71]\n\t"
      "v_exp_f32 v19, v19\n\t"
      "v_mfma_f32_16x16x32_bf16 v[72:75], v[196:199], v[36:39], v[72:75]\n\t"
      "v_cvt_pk_bf16_f32 v14, v16, v17\n\t"
      "v_cvt_pk_bf16_f32 v15, v18, v19\n\t"
      "v_mfma_f32_32x32x16_bf16 v[100:115], a[4:7], a[20:23], v[100:115]\n\t"
      "v_rcp_f32 v2, v176\n\t"
      "v_subrev_u32 v8, 0x2b800000, v176\n\t"
      "v_max_u32 v233, v233, v8\n\t"
      "v_exp_f32 v52, v52\n\t"
      "v_mfma_f32_32x32x16_bf16 v[100:115], a[8:11], a[136:139], v[100:115]\n\t"
      "v_exp_f32 v53, v53\n\t"
      "v_exp_f32 v54, v54\n\t"
      "v_exp_f32 v55, v55\n\t"
      "v_mfma_f32_32x32x16_bf16 v[100:115], a[12:15], a[140:143], v[100:115]\n\t"
      "s_waitcnt lgkmcnt(1)\n\t"
      "global_store_dwordx4 v232, a[28:31], s[48:49] sc1 nt\n\t"
      "v_cvt_pk_bf16_f32 v52, v52, v53\n\t"
      "s_waitcnt lgkmcnt(0)\n\t"
      "global_store_dwordx4 v232, a[32:35], s[48:49] offset:2048 sc1 nt\n\t"
      "v_mfma_f32_32x32x16_bf16 v[36:51], a[36:39], a[16:19], 0\n\t"
      "v_cvt_pk_bf16_f32 v53, v54, v55\n\t"
      "ds_write_b64 v238, v[52:53] offset:0\n\t"
      "v_exp_f32 v56, v56\n\t"
      "v_mfma_f32_32x32x16_bf16 v[36:51], a[40:43], a[20:23], v[36:51]\n\t"
      "v_exp_f32 v57, v57\n\t"
      "v_exp_f32 v58, v58\n\t"
      "v_cvt_pk_bf16_f32 v160, v100, v101\n\t"
      "v_cvt_pk_bf16_f32 v161, v102, v103\n\t"
      "v_mfma_f32_32x32x16_bf16 v[36:51], a[44:47], a[136:139], v[36:51]\n\t"
      "v_cvt_pk_bf16_f32 v162, v104, v105\n\t"
      "v_cvt_pk_bf16_f32 v163, v106, v107\n\t"
      "v_cvt_pk_bf16_f32 v164, v108, v109\n\t"
      "v_cvt_pk_bf16_f32 v165, v110, v111\n\t"
      "v_cvt_pk_bf16_f32 v166, v112, v113\n\t"
      "v_cvt_pk_bf16_f32 v167, v114, v115\n\t"
      "v_mfma_f32_32x32x16_bf16 v[36:51], a[48:51], a[140:143], v[36:51]\n\t"
      "v_exp_f32 v59, v59\n\t"
      "v_cvt_pk_bf16_f32 v56, v56, v57\n\t"
      "v_cvt_pk_bf16_f32 v57, v58, v59\n\t"
      "ds_write_b64 v238, v[56:57] offset:64\n\t"
      "v_mfma_f32_32x32x16_bf16 v[84:99], a[124:127], a[76:79], v[84:99]\n\t"
      "v_exp_f32 v68, v68\n\t"
      "v_exp_f32 v69, v69\n\t"
      "v_exp_f32 v70, v70\n\t"
      "v_mfma_f32_32x32x16_bf16 v[84:99], a[128:131], a[80:83], v[84:99]\n\t"
      "v_exp_f32 v71, v71\n\t"
      "v_cvt_pk_bf16_f32 v68, v68, v69\n\t"
      "v_cvt_pk_bf16_f32 v69, v70, v71\n\t"
      "v_cvt_pk_bf16_f32 v176, v36, v37\n\t"
      "v_cvt_pk_bf16_f32 v177, v38, v39\n\t"
      "v_mfma_f32_32x32x16_bf16 v[52:67], v[144:147], v[4:7], 0\n\t"
      "v_cvt_pk_bf16_f32 v178, v40, v41\n\t"
      "v_cvt_pk_bf16_f32 v179, v42, v43\n\t"
      "v_cvt_pk_bf16_f32 v220, v44, v45\n\t"
      "v_cvt_pk_bf16_f32 v221, v46, v47\n\t"
      "v_cvt_pk_bf16_f32 v222, v48, v49\n\t"
      "v_cvt_pk_bf16_f32 v223, v50, v51\n\t"
      "v_mfma_f32_16x16x32_bf16 v[100:103], a[24:27], v[4:7], 0\n\t"
      "v_fmac_f32 v84, v20, v2\n\t"
      "v_fmac_f32 v85, v21, v2\n\t"
      "v_mfma_f32_32x32x16_bf16 v[52:67], v[156:159], v[12:15], v[52:67]\n\t"
      "v_cvt_pk_bf16_f32 v84, v84, v85\n\t"
      "v_pk_max_i16 v84, v84, 0\n\t"
      "v_fmac_f32 v86, v22, v2\n\t"
      "v_fmac_f32 v87, v23, v2\n\t"
      "v_cvt_pk_bf16_f32 v85, v86, v87\n\t"
      "v_pk_max_i16 v85, v85, 0\n\t"
      "v_mfma_f32_16x16x32_bf16 v[100:103], a[24:27], v[12:15], v[100:103]\n\t"
      "v_fmac_f32 v88, v24, v2\n\t"
      "v_fmac_f32 v89, v25, v2\n\t"
      "v_mfma_f32_32x32x16_bf16 v[4:19], v[184:187], v[208:211], 0\n\t"
      "v_cvt_pk_bf16_f32 v86, v88, v89\n\t"
      "v_pk_max_i16 v86, v86, 0\n\t"
      "v_fmac_f32 v90, v26, v2\n\t"
      "v_fmac_f32 v91, v27, v2\n\t"
      "v_cvt_pk_bf16_f32 v87, v90, v91\n\t"
      "v_pk_max_i16 v87, v87, 0\n\t"
      "v_mfma_f32_32x32x16_bf16 v[4:19], v[188:191], v[212:215], v[4:19]\n\t"
      "v_fmac_f32 v92, v28, v2\n\t"
      "v_fmac_f32 v93, v29, v2\n\t"
      "v_cvt_pk_bf16_f32 v88, v92, v93\n\t"
      "v_pk_max_i16 v88, v88, 0\n\t"
      "v_fmac_f32 v94, v30, v2\n\t"
      "v_fmac_f32 v95, v31, v2\n\t"
      "v_mfma_f32_16x16x32_bf16 v[104:107], v[192:195], v[208:211], 0\n\t"
      "v_cvt_pk_bf16_f32 v89, v94, v95\n\t"
      "v_pk_max_i16 v89, v89, 0\n\t"
      "v_mfma_f32_16x16x32_bf16 v[104:107], v[196:199], v[212:215], v[104:107]\n\t"
      "v_fmac_f32 v96, v32, v2\n\t"
      "v_fmac_f32 v97, v33, v2\n\t"
      "v_mfma_f32_32x32x16_bf16 v[36:51], a[16:19], a[52:55], 0\n\t"
      "v_cvt_pk_bf16_f32 v90, v96, v97\n\t"
      "v_pk_max_i16 v90, v90, 0\n\t"
      "v_fmac_f32 v98, v34, v2\n\t"
      "v_fmac_f32 v99, v35, v2\n\t"
      "v_cvt_pk_bf16_f32 v91, v98, v99\n\t"
      "v_pk_max_i16 v91, v91, 0\n\t"
      "v_mfma_f32_32x32x16_bf16 v[36:51], a[20:23], a[56:59], v[36:51]\n\t"
      "ds_write_b128 v235, v[84:87]\n\t"
      "v_mfma_f32_32x32x16_bf16 v[36:51], a[136:139], a[60:63], v[36:51]\n\t"
      "ds_write_b128 v236, v[88:91]\n\t"
      "v_mfma_f32_32x32x16_bf16 v[36:51], a[140:143], a[64:67], v[36:51]\n\t"
      "v_exp_f32 v72, v72\n\t"
      "v_exp_f32 v73, v73\n\t"
      "ds_read_b128 a[28:31], v237\n\t"
      "v_exp_f32 v116, v116\n\t"
      "v_mfma_f32_32x32x16_bf16 v[20:35], a[0:3], a[144:147], 0\n\t"
      "ds_read_b128 a[32:35], v237 offset:1024\n\t"
      "v_cvt_pk_bf16_f32 v72, v72, v73\n\t"
      "v_mov_b32 v73, 0\n\t"
      "ds_write_b64 v238, v[68:69] offset:128\n\t"
      "v_mfma_f32_32x32x16_bf16 v[20:35], a[4:7], a[148:151], v[20:35]\n\t"
      "v_exp_f32 v117, v117\n\t"
      "ds_write_b64 v239, v[72:73] offset:192\n\t"
      "v_cvt_pk_bf16_f32 v184, v36, v37\n\t"
      "v_mfma_f32_32x32x16_bf16 v[20:35], a[8:11], a[152:155], v[20:35]\n\t"
      "ds_read_b128 a[68:71], v1 offset:0\n\t"
      "v_cvt_pk_bf16_f32 v185, v38, v39\n\t"
      "v_cvt_pk_bf16_f32 v186, v40, v41\n\t"
      "ds_read_b128 a[72:75], v1 offset:32\n\t"
      "v_cvt_pk_bf16_f32 v187, v42, v43\n\t"
      "v_cvt_pk_bf16_f32 v188, v44, v45\n\t"
      "v_mfma_f32_32x32x16_bf16 v[20:35], a[12:15], a[156:159], v[20:35]\n\t"
      "ds_read_b128 a[76:79], v1 offset:64\n\t"
      "v_cvt_pk_bf16_f32 v189, v46, v47\n\t"
      "ds_read_b128 a[80:83], v1 offset:96\n\t"
      "v_cvt_pk_bf16_f32 v190, v48, v49\n\t"
      "v_cvt_pk_bf16_f32 v191, v50, v51\n\t"
      "v_cvt_pk_bf16_f32 v116, v116, v117\n\t"
      "v_mfma_f32_32x32x16_bf16 v[36:51], a[116:119], a[84:87], 0\n\t"
      "s_waitcnt lgkmcnt(7)\n\t"
      "global_store_dwordx4 v232, a[28:31], s[50:51] sc1 nt\n\t"
      "s_waitcnt lgkmcnt(6)\n\t"
      "global_store_dwordx4 v232, a[32:35], s[50:51] offset:2048 sc1 nt\n\t"
      "v_mfma_f32_32x32x16_bf16 v[36:51], a[120:123], a[88:91], v[36:51]\n\t"
      "ds_read_b128 a[28:31], v1 offset:128\n\t"
      "ds_read_b128 a[32:35], v1 offset:160\n\t"
      "ds_read_b128 a[84:87], v1 offset:192\n\t"
      "v_cvt_pk_bf16_f32 v192, v20, v21\n\t"
      "v_cvt_pk_bf16_f32 v193, v22, v23\n\t"
      "v_cvt_pk_bf16_f32 v194, v24, v25\n\t"
      "v_mfma_f32_32x32x16_bf16 v[36:51], a[124:127], a[92:95], v[36:51]\n\t"
      "v_cvt_pk_bf16_f32 v195, v26, v27\n\t"
      "v_cvt_pk_bf16_f32 v196, v28, v29\n\t"
      "v_cvt_pk_bf16_f32 v197, v30, v31\n\t"
      "v_cvt_pk_bf16_f32 v198, v32, v33\n\t"
      "v_cvt_pk_bf16_f32 v199, v34, v35\n\t"
      "s_waitcnt lgkmcnt(5)\n\t"
      "v_mfma_f32_16x16x32_bf16 v[84:87], v[144:147], a[68:71], 0\n\t"
      "v_exp_f32 v118, v118\n\t"
      "v_mfma_f32_32x32x16_bf16 v[36:51], a[128:131], a[96:99], v[36:51]\n\t"
      "v_exp_f32 v119, v119\n\t"
      "v_exp_f32 v120, v120\n\t"
      "v_exp_f32 v121, v121\n\t"
      "v_mfma_f32_32x32x16_bf16 v[20:35], a[36:39], a[144:147], 0\n\t"
      "v_exp_f32 v122, v122\n\t"
      "v_exp_f32 v123, v123\n\t"
      "v_cvt_pk_bf16_f32 v117, v118, v119\n\t"
      "v_cvt_pk_bf16_f32 v118, v120, v121\n\t"
      "v_mfma_f32_32x32x16_bf16 v[20:35], a[40:43], a[148:151], v[20:35]\n\t"
      "v_cvt_pk_bf16_f32 v119, v122, v123\n\t"
      "v_exp_f32 v124, v124\n\t"
      "v_exp_f32 v125, v125\n\t"
      "v_exp_f32 v126, v126\n\t"
      "v_mfma_f32_32x32x16_bf16 v[20:35], a[44:47], a[152:155], v[20:35]\n\t"
      "v_exp_f32 v127, v127\n\t"
      "v_exp_f32 v128, v128\n\t"
      "v_exp_f32 v129, v129\n\t"
      "v_mfma_f32_32x32x16_bf16 v[20:35], a[48:51], a[156:159], v[20:35]\n\t"
      "v_exp_f32 v130, v130\n\t"
      "v_exp_f32 v131, v131\n\t"
      "v_cvt_pk_bf16_f32 v124, v124, v125\n\t"
      "v_cvt_pk_bf16_f32 v125, v126, v127\n\t"
      "v_mfma_f32_32x32x16_bf16 v[68:83], v[160:163], v[176:179], 0\n\t"
      "v_cvt_pk_bf16_f32 v126, v128, v129\n\t"
      "v_cvt_pk_bf16_f32 v127, v130, v131\n\t"
      "ds_read_b128 a[88:91], v3 offset:34816\n\t"
      "ds_read_b128 a[92:95], v152 offset:34816\n\t"
      "ds_read_b128 a[96:99], v224 offset:34816\n\t"
      "v_exp_f32 v104, v104\n\t"
      "v_mfma_f32_32x32x16_bf16 v[68:83], v[164:167], v[220:223], v[68:83]\n\t"
      "v_cvt_pk_bf16_f32 v208, v20, v21\n\t"
      "v_cvt_pk_bf16_f32 v209, v22, v23\n\t"
      "v_cvt_pk_bf16_f32 v210, v24, v25\n\t"
      "v_cvt_pk_bf16_f32 v211, v26, v27\n\t"
      "v_cvt_pk_bf16_f32 v212, v28, v29\n\t"
      "v_cvt_pk_bf16_f32 v213, v30, v31\n\t"
      "v_mfma_f32_32x32x16_bf16 v[52:67], v[168:171], v[116:119], v[52:67]\n\t"
      "v_cvt_pk_bf16_f32 v214, v32, v33\n\t"
      "v_cvt_pk_bf16_f32 v215, v34, v35\n\t"
      "ds_read_b128 a[100:103], v225 offset:34816\n\t"
      "ds_read_b128 a[104:107], v226 offset:26624\n\t"
      "ds_read_b128 a[108:111], v227 offset:26624\n\t"
      "v_exp_f32 v105, v105\n\t"
      "v_mfma_f32_16x16x32_bf16 v[100:103], a[24:27], v[116:119], v[100:103]\n\t"
      "ds_read_b128 a[112:115], v228 offset:26624\n\t"
      "v_cvt_pk_bf16_f32 v148, v104, v105\n\t"
      "v_mfma_f32_32x32x16_bf16 v[52:67], v[172:175], v[124:127], v[52:67]\n\t"
      "ds_read_b128 a[160:163], v229 offset:26624\n\t"
      "v_exp_f32 v4, v4\n\t"
      "v_exp_f32 v5, v5\n\t"
      "v_exp_f32 v6, v6\n\t"
      "v_mfma_f32_16x16x32_bf16 v[100:103], a[24:27], v[124:127], v[100:103]\n\t"
      "v_exp_f32 v7, v7\n\t"
      "v_mfma_f32_16x16x32_bf16 v[88:91], a[132:135], a[68:71], 0\n\t"
      "v_exp_f32 v8, v8\n\t"
      "v_mfma_f32_16x16x32_bf16 v[84:87], v[156:159], a[72:75], v[84:87]\n\t"
      "v_exp_f32 v9, v9\n\t"
      "v_mfma_f32_16x16x32_bf16 v[88:91], a[132:135], a[72:75], v[88:91]\n\t"
      "v_cvt_pk_bf16_f32 v4, v4, v5\n\t"
      "s_waitcnt lgkmcnt(4)\n\t"
      "v_mfma_f32_16x16x32_bf16 v[84:87], v[168:171], a[76:79], v[84:87]\n\t"
      "v_exp_f32 v10, v10\n\t"
      "v_mfma_f32_16x16x32_bf16 v[88:91], a[132:135], a[76:79], v[88:91]\n\t"
      "v_exp_f32 v11, v11\n\t"
      "v_mfma_f32_16x16x32_bf16 v[84:87], v[172:175], a[80:83], v[84:87]\n\t"
      "v_cvt_pk_bf16_f32 v5, v6, v7\n\t"
      "v_cvt_pk_bf16_f32 v6, v8, v9\n\t"
      "v_mfma_f32_16x16x32_bf16 v[88:91], a[132:135], a[80:83], v[88:91]\n\t"
      "v_cvt_pk_bf16_f32 v7, v10, v11\n\t"
      "v_exp_f32 v12, v12\n\t"
      "v_mfma_f32_16x16x32_bf16 v[84:87], v[200:203], a[28:31], v[84:87]\n\t"
      "v_exp_f32 v13, v13\n\t"
      "v_mfma_f32_16x16x32_bf16 v[88:91], a[132:135], a[28:31], v[88:91]\n\t"
      "v_exp_f32 v14, v14\n\t"
      "v_mfma_f32_16x16x32_bf16 v[84:87], v[204:207], a[32:35], v[84:87]\n\t"
      "v_exp_f32 v15, v15\n\t"
      "v_mfma_f32_16x16x32_bf16 v[88:91], a[132:135], a[32:35], v[88:91]\n\t"
      "v_exp_f32 v16, v16\n\t"
      "v_mfma_f32_16x16x32_bf16 v[84:87], v[132:135], a[84:87], v[84:87]\n\t"
      "v_exp_f32 v17, v17\n\t"
      "v_mfma_f32_16x16x32_bf16 v[88:91], a[132:135], a[84:87], v[88:91]\n\t"
      "v_exp_f32 v18, v18\n\t"
      "v_mfma_f32_32x32x16_bf16 v[20:35], a[144:147], a[52:55], 0\n\t"
      "v_exp_f32 v19, v19\n\t"
      "v_cvt_pk_bf16_f32 v12, v12, v13\n\t"
      "v_cvt_pk_bf16_f32 v13, v14, v15\n\t"
      "v_cvt_pk_bf16_f32 v14, v16, v17\n\t"
      "v_cvt_pk_bf16_f32 v15, v18, v19\n\t"
      "v_mfma_f32_32x32x16_bf16 v[20:35], a[148:151], a[56:59], v[20:35]\n\t"
      "v_rcp_f32 v2, v88\n\t"
      "s_mov_b64 exec, s[54:55]\n\tv_subrev_u32 v3, 0x2b800000, v88\n\tv_max_u32 v233, v233, v3\n\ts_mov_b64 exec, -1\n\t"
      "v_fma_f32 v84, v84, v2, v216\n\t"
      "v_fma_f32 v85, v85, v2, v217\n\t"
      "v_mfma_f32_32x32x16_bf16 v[20:35], a[152:155], a[60:63], v[20:35]\n\t"
      "v_cvt_pk_bf16_f32 v84, v84, v85\n\t"
      "v_pk_max_i16 v84, v84, 0\n\t"
      "v_fma_f32 v86, v86, v2, v218\n\t"
      "v_fma_f32 v87, v87, v2, v219\n\t"
      "v_cvt_pk_bf16_f32 v85, v86, v87\n\t"
      "v_pk_max_i16 v85, v85, 0\n\t"
      "v_mfma_f32_32x32x16_bf16 v[20:35], a[156:159], a[64:67], v[20:35]\n\t"
      "s_mov_b64 exec, s[54:55]\n\tglobal_store_dwordx2 v240, v[84:85], s[48:49] sc1 nt\n\ts_mov_b64 exec, -1\n\t"
      "v_exp_f32 v68, v68\n\t"
      "v_exp_f32 v69, v69\n\t"
      "v_mfma_f32_32x32x16_bf16 v[84:99], v[192:195], v[176:179], 0\n\t"
      "v_exp_f32 v70, v70\n\t"
      "v_exp_f32 v71, v71\n\t"
      "v_exp_f32 v72, v72\n\t"
      "v_mfma_f32_32x32x16_bf16 v[84:99], v[196:199], v[220:223], v[84:99]\n\t"
      "v_exp_f32 v73, v73\n\t"
      "v_exp_f32 v74, v74\n\t"
      "v_exp_f32 v75, v75\n\t"
      "v_mfma_f32_32x32x16_bf16 v[52:67], v[132:135], v[148:151], v[52:67]\n\t"
      "v_cvt_pk_bf16_f32 v156, v20, v21\n\t"
      "v_cvt_pk_bf16_f32 v157, v22, v23\n\t"
      "v_cvt_pk_bf16_f32 v158, v24, v25\n\t"
      "v_cvt_pk_bf16_f32 v159, v26, v27\n\t"
      "v_cvt_pk_bf16_f32 v168, v28, v29\n\t"
      "v_cvt_pk_bf16_f32 v169, v30, v31\n\t"
      "v_mfma_f32_16x16x32_bf16 v[100:103], a[24:27], v[148:151], v[100:103]\n\t"
      "v_cvt_pk_bf16_f32 v170, v32, v33\n\t"
      "v_cvt_pk_bf16_f32 v171, v34, v35\n\t"
      "v_mfma_f32_32x32x16_bf16 v[52:67], v[200:203], v[4:7], v[52:67]\n\t"
      "v_cvt_pk_bf16_f32 v68, v68, v69\n\t"
      "v_cvt_pk_bf16_f32 v69, v70, v71\n\t"
      "v_cvt_pk_bf16_f32 v70, v72, v73\n\t"
      "v_cvt_pk_bf16_f32 v71, v74, v75\n\t"
      "v_exp_f32 v76, v76\n\t"
      "v_mfma_f32_16x16x32_bf16 v[100:103], a[24:27], v[4:7], v[100:103]\n\t"
      "v_exp_f32 v77, v77\n\t"
      "v_mfma_f32_32x32x16_bf16 v[52:67], v[204:207], v[12:15], v[52:67]\n\t"
      "v_exp_f32 v78, v78\n\t"
      "v_exp_f32 v79, v79\n\t"
      "v_exp_f32 v80, v80\n\t"
      "v_mfma_f32_16x16x32_bf16 v[100:103], a[24:27], v[12:15], v[100:103]\n\t"
      "v_exp_f32 v81, v81\n\t"
      "v_mfma_f32_32x32x16_bf16 v[4:19], v[184:187], v[68:71], 0\n\t"
      "v_exp_f32 v82, v82\n\t"
      "v_exp_f32 v83, v83\n\t"
      "v_cvt_pk_bf16_f32 v76, v76, v77\n\t"
      "v_cvt_pk_bf16_f32 v77, v78, v79\n\t"
      "v_mfma_f32_16x16x32_bf16 v[172:175], a[24:27], v[68:71], 0\n\t"
      "v_cvt_pk_bf16_f32 v78, v80, v81\n\t"
      "v_subrev_u32 v2, 0x2b800000, v100\n\t"
      "v_mfma_f32_32x32x16_bf16 v[20:35], v[160:163], v[208:211], 0\n\t"
      "v_rcp_f32 v3, v100\n\t"
      "v_max_u32 v233, v233, v2\n\t"
      "v_fmac_f32 v36, v52, v3\n\t"
      "v_fmac_f32 v37, v53, v3\n\t"
      "v_cvt_pk_bf16_f32 v36, v36, v37\n\t"
      "v_mfma_f32_32x32x16_bf16 v[20:35], v[164:167], v[212:215], v[20:35]\n\t"
      "v_pk_max_i16 v36, v36, 0\n\t"
      "v_fmac_f32 v38, v54, v3\n\t"
      "v_fmac_f32 v39, v55, v3\n\t"
      "v_cvt_pk_bf16_f32 v37, v38, v39\n\t"
      "v_pk_max_i16 v37, v37, 0\n\t"
      "v_fmac_f32 v40, v56, v3\n\t"
      "v_mfma_f32_32x32x16_bf16 v[100:115], a[0:3], a[88:91], 0\n\t"
      "v_fmac_f32 v41, v57, v3\n\t"
      "v_cvt_pk_bf16_f32 v38, v40, v41\n\t"
      "v_pk_max_i16 v38, v38, 0\n\t"
      "v_fmac_f32 v42, v58, v3\n\t"
      "v_fmac_f32 v43, v59, v3\n\t"
      "v_cvt_pk_bf16_f32 v39, v42, v43\n\t"
      "v_mfma_f32_32x32x16_bf16 v[100:115], a[4:7], a[92:95], v[100:115]\n\t"
      "v_pk_max_i16 v39, v39, 0\n\t"
      "v_fmac_f32 v44, v60, v3\n\t"
      "v_fmac_f32 v45, v61, v3\n\t"
      "v_cvt_pk_bf16_f32 v40, v44, v45\n\t"
      "v_pk_max_i16 v40, v40, 0\n\t"
      "v_fmac_f32 v46, v62, v3\n\t"
      "v_mfma_f32_32x32x16_bf16 v[100:115], a[8:11], a[96:99], v[100:115]\n\t"
      "v_fmac_f32 v47, v63, v3\n\t"
      "v_cvt_pk_bf16_f32 v41, v46, v47\n\t"
      "v_pk_max_i16 v41, v41, 0\n\t"
      "v_fmac_f32 v48, v64, v3\n\t"
      "v_fmac_f32 v49, v65, v3\n\t"
      "v_cvt_pk_bf16_f32 v42, v48, v49\n\t"
      "v_mfma_f32_32x32x16_bf16 v[100:115], a[12:15], a[100:103], v[100:115]\n\t"
      "v_pk_max_i16 v42, v42, 0\n\t"
      "v_fmac_f32 v50, v66, v3\n\t"
      "v_fmac_f32 v51, v67, v3\n\t"
      "v_cvt_pk_bf16_f32 v43, v50, v51\n\t"
      "v_pk_max_i16 v43, v43, 0\n\t"
      "ds_write_b128 v235, v[36:39]\n\t"
      "v_mfma_f32_32x32x16_bf16 v[44:59], v[192:195], v[208:211], 0\n\t"
      "ds_write_b128 v236, v[40:43]\n\t"
      "v_mfma_f32_32x32x16_bf16 v[44:59], v[196:199], v[212:215], v[44:59]\n\t"
      "v_cvt_pk_bf16_f32 v79, v82, v83\n\t"
      "v_exp_f32 v84, v84\n\t"
      "v_exp_f32 v85, v85\n\t"
      "ds_read_b128 a[28:31], v237\n\t"
      "v_mfma_f32_32x32x16_bf16 v[4:19], v[188:191], v[76:79], v[4:19]\n\t"
      "ds_read_b128 a[32:35], v237 offset:1024\n\t"
      "v_exp_f32 v86, v86\n\t"
      "v_exp_f32 v87, v87\n\t"
      "s_waitcnt lgkmcnt(4)\n\t"
      "v_mfma_f32_32x32x16_bf16 v[60:75], a[0:3], a[104:107], 0\n\t"
      "v_exp_f32 v88, v88\n\t"
      "v_exp_f32 v89, v89\n\t"
      "v_exp_f32 v90, v90\n\t"
      "v_mfma_f32_16x16x32_bf16 v[172:175], a[24:27], v[76:79], v[172:175]\n\t"
      "v_exp_f32 v91, v91\n\t"
      "v_mfma_f32_32x32x16_bf16 v[116:131], a[88:91], a[52:55], 0\n\t"
      "v_cvt_pk_bf16_f32 v84, v84, v85\n\t"
      "v_cvt_pk_bf16_f32 v85, v86, v87\n\t"
      "v_cvt_pk_bf16_f32 v86, v88, v89\n\t"
      "v_cvt_pk_bf16_f32 v87, v90, v91\n\t"
      "v_exp_f32 v92, v92\n\t"
      "v_mfma_f32_32x32x16_bf16 v[116:131], a[92:95], a[56:59], v[116:131]\n\t"
      "v_exp_f32 v93, v93\n\t"
      "v_exp_f32 v94, v94\n\t"
      "v_exp_f32 v95, v95\n\t"
      "v_mfma_f32_32x32x16_bf16 v[4:19], v[156:159], v[84:87], v[4:19]\n\t"
      "s_waitcnt lgkmcnt(1)\n\t"
      "global_store_dwordx4 v232, a[28:31], s[52:53] sc1 nt\n\t"
      "s_waitcnt lgkmcnt(0)\n\t"
      "global_store_dwordx4 v232, a[32:35], s[52:53] offset:2048 sc1 nt\n\t"
      "v_mfma_f32_16x16x32_bf16 v[172:175], a[24:27], v[84:87], v[172:175]\n\t"
      "v_exp_f32 v96, v96\n\t"
      "v_mfma_f32_32x32x16_bf16 v[116:131], a[96:99], a[60:63], v[116:131]\n\t"
      "v_exp_f32 v97, v97\n\t"
      "v_exp_f32 v98, v98\n\t"
      "v_exp_f32 v99, v99\n\t"
      "v_mfma_f32_32x32x16_bf16 v[116:131], a[100:103], a[64:67], v[116:131]\n\t"
      "v_cvt_pk_bf16_f32 v92, v92, v93\n\t"
      "v_cvt_pk_bf16_f32 v93, v94, v95\n\t"
      "v_cvt_pk_bf16_f32 v94, v96, v97\n\t"
      "v_cvt_pk_bf16_f32 v95, v98, v99\n\t"
      "v_cvt_pk_bf16_f32 v200, v100, v101\n\t"
      "v_cvt_pk_bf16_f32 v201, v102, v103\n\t"
      "v_mfma_f32_32x32x16_bf16 v[4:19], v[168:171], v[92:95], v[4:19]\n\t"
      "v_cvt_pk_bf16_f32 v202, v104, v105\n\t"
      "v_cvt_pk_bf16_f32 v203, v106, v107\n\t"
      "v_cvt_pk_bf16_f32 v204, v108, v109\n\t"
      "v_cvt_pk_bf16_f32 v205, v110, v111\n\t"
      "v_cvt_pk_bf16_f32 v206, v112, v113\n\t"
      "v_cvt_pk_bf16_f32 v207, v114, v115\n\t"
      "v_mfma_f32_16x16x32_bf16 v[172:175], a[24:27], v[92:95], v[172:175]\n\t"
      "v_exp_f32 v20, v20\n\t"
      "v_mfma_f32_32x32x16_bf16 v[76:91], v[200:203], v[176:179], 0\n\t"
      "v_exp_f32 v21, v21\n\t"
      "v_exp_f32 v22, v22\n\t"
      "v_exp_f32 v23, v23\n\t"
      "v_mfma_f32_32x32x16_bf16 v[76:91], v[204:207], v[220:223], v[76:91]\n\t"
      "v_exp_f32 v24, v24\n\t"
      "v_exp_f32 v25, v25\n\t"
      "v_exp_f32 v26, v26\n\t"
      "v_mfma_f32_32x32x16_bf16 v[60:75], a[4:7], a[108:111], v[60:75]\n\t"
      "v_exp_f32 v27, v27\n\t"
      "v_cvt_pk_bf16_f32 v20, v20, v21\n\t"
      "v_cvt_pk_bf16_f32 v21, v22, v23\n\t"
      "v_cvt_pk_bf16_f32 v22, v24, v25\n\t"
      "v_cvt_pk_bf16_f32 v23, v26, v27\n\t"
      "v_mfma_f32_32x32x16_bf16 v[60:75], a[8:11], a[112:115], v[60:75]\n\t"
      "v_exp_f32 v28, v28\n\t"
      "v_exp_f32 v29, v29\n\t"
      "v_exp_f32 v30, v30\n\t"
      "v_mfma_f32_32x32x16_bf16 v[92:107], v[184:187], v[20:23], 0\n\t"
      "v_exp_f32 v31, v31\n\t"
      "v_exp_f32 v32, v32\n\t"
      "v_exp_f32 v33, v33\n\t"
      "v_mfma_f32_16x16x32_bf16 v[216:219], a[24:27], v[20:23], 0\n\t"
      "v_exp_f32 v34, v34\n\t"
      "v_mfma_f32_32x32x16_bf16 v[60:75], a[12:15], a[160:163], v[60:75]\n\t"
      "v_exp_f32 v35, v35\n\t"
      "v_cvt_pk_bf16_f32 v28, v28, v29\n\t"
      "v_cvt_pk_bf16_f32 v29, v30, v31\n\t"
      "v_cvt_pk_bf16_f32 v30, v32, v33\n\t"
      "v_cvt_pk_bf16_f32 v31, v34, v35\n\t"
      "v_mfma_f32_32x32x16_bf16 v[132:147], v[200:203], v[208:211], 0\n\t"
      "v_cvt_pk_bf16_f32 v224, v116, v117\n\t"
      "v_cvt_pk_bf16_f32 v225, v118, v119\n\t"
      "v_cvt_pk_bf16_f32 v226, v120, v121\n\t"
      "v_cvt_pk_bf16_f32 v227, v122, v123\n\t"
      "v_cvt_pk_bf16_f32 v228, v124, v125\n\t"
      "v_cvt_pk_bf16_f32 v229, v126, v127\n\t"
      "v_mfma_f32_32x32x16_bf16 v[92:107], v[188:191], v[28:31], v[92:107]\n\t"
      "v_cvt_pk_bf16_f32 v230, v128, v129\n\t"
      "v_cvt_pk_bf16_f32 v231, v130, v131\n\t"
      "v_exp_f32 v44, v44\n\t"
      "v_exp_f32 v45, v45\n\t"
      "v_mfma_f32_16x16x32_bf16 v[216:219], a[24:27], v[28:31], v[216:219]\n\t"
      "v_exp_f32 v46, v46\n\t"
      "v_mfma_f32_32x32x16_bf16 v[132:147], v[204:207], v[212:215], v[132:147]\n\t"
      "v_exp_f32 v47, v47\n\t"
      "v_exp_f32 v48, v48\n\t"
      "v_exp_f32 v49, v49\n\t"
      "v_mfma_f32_16x16x32_bf16 v[36:39], a[104:107], a[52:55], 0\n\t"
      "v_exp_f32 v50, v50\n\t"
      "v_mfma_f32_16x16x32_bf16 v[36:39], a[108:111], a[56:59], v[36:39]\n\t"
      "v_exp_f32 v51, v51\n\t"
      "v_mfma_f32_16x16x32_bf16 v[36:39], a[112:115], a[60:63], v[36:39]\n\t"
      "v_cvt_pk_bf16_f32 v44, v44, v45\n\t"
      "v_cvt_pk_bf16_f32 v45, v46, v47\n\t"
      "v_mfma_f32_16x16x32_bf16 v[36:39], a[160:163], a[64:67], v[36:39]\n\t"
      "v_cvt_pk_bf16_f32 v46, v48, v49\n\t"
      "v_cvt_pk_bf16_f32 v47, v50, v51\n\t"
      "v_mfma_f32_32x32x16_bf16 v[20:35], a[36:39], a[88:91], 0\n\t"
      "v_exp_f32 v52, v52\n\t"
      "v_exp_f32 v53, v53\n\t"
      "v_exp_f32 v54, v54\n\t"
      "v_mfma_f32_32x32x16_bf16 v[92:107], v[156:159], v[44:47], v[92:107]\n\t"
      "v_exp_f32 v55, v55\n\t"
      "v_exp_f32 v56, v56\n\t"
      "v_exp_f32 v57, v57\n\t"
      "v_mfma_f32_16x16x32_bf16 v[216:219], a[24:27], v[44:47], v[216:219]\n\t"
      "v_exp_f32 v58, v58\n\t"
      "v_mfma_f32_32x32x16_bf16 v[20:35], a[40:43], a[92:95], v[20:35]\n\t"
      "v_exp_f32 v59, v59\n\t"
      "v_cvt_pk_bf16_f32 v52, v52, v53\n\t"
      "v_cvt_pk_bf16_f32 v53, v54, v55\n\t"
      "v_cvt_pk_bf16_f32 v54, v56, v57\n\t"
      "v_cvt_pk_bf16_f32 v55, v58, v59\n\t"
      "v_mfma_f32_32x32x16_bf16 v[20:35], a[44:47], a[96:99], v[20:35]\n\t"
      "v_cvt_pk_bf16_f32 v124, v60, v61\n\t"
      "v_cvt_pk_bf16_f32 v125, v62, v63\n\t"
      "v_cvt_pk_bf16_f32 v126, v64, v65\n\t"
      "v_cvt_pk_bf16_f32 v127, v66, v67\n\t"
      "v_cvt_pk_bf16_f32 v128, v68, v69\n\t"
      "v_cvt_pk_bf16_f32 v129, v70, v71\n\t"
      "v_mfma_f32_32x32x16_bf16 v[92:107], v[168:171], v[52:55], v[92:107]\n\t"
      "v_cvt_pk_bf16_f32 v130, v72, v73\n\t"
      "v_cvt_pk_bf16_f32 v131, v74, v75\n\t"
      "v_cvt_pk_bf16_f32 v152, v36, v37\n\t"
      "v_exp_f32 v76, v76\n\t"
      "v_exp_f32 v77, v77\n\t"
      "v_mfma_f32_16x16x32_bf16 v[216:219], a[24:27], v[52:55], v[216:219]\n\t"
      "v_exp_f32 v78, v78\n\t"
      "v_mfma_f32_16x16x32_bf16 v[52:55], v[124:127], v[176:179], 0\n\t"
      "v_exp_f32 v79, v79\n\t"
      "v_mfma_f32_16x16x32_bf16 v[52:55], v[128:131], v[220:223], v[52:55]\n\t"
      "v_exp_f32 v80, v80\n\t"
      "v_mfma_f32_16x16x32_bf16 v[68:71], v[124:127], v[208:211], 0\n\t"
      "v_exp_f32 v81, v81\n\t"
      "v_mfma_f32_16x16x32_bf16 v[68:71], v[128:131], v[212:215], v[68:71]\n\t"
      "v_exp_f32 v82, v82\n\t"
      "v_mfma_f32_32x32x16_bf16 v[20:35], a[48:51], a[100:103], v[20:35]\n\t"
      "v_exp_f32 v83, v83\n\t"
      "v_cvt_pk_bf16_f32 v76, v76, v77\n\t"
      "v_cvt_pk_bf16_f32 v77, v78, v79\n\t"
      "v_cvt_pk_bf16_f32 v78, v80, v81\n\t"
      "v_cvt_pk_bf16_f32 v79, v82, v83\n\t"
      "v_mfma_f32_32x32x16_bf16 v[36:51], a[116:119], a[16:19], 0\n\t"
      "v_exp_f32 v84, v84\n\t"
      "v_exp_f32 v85, v85\n\t"
      "v_exp_f32 v86, v86\n\t"
      "v_mfma_f32_32x32x16_bf16 v[4:19], v[224:227], v[76:79], v[4:19]\n\t"
      "v_exp_f32 v87, v87\n\t"
      "v_exp_f32 v88, v88\n\t"
      "v_exp_f32 v89, v89\n\t"
      "v_mfma_f32_16x16x32_bf16 v[172:175], a[24:27], v[76:79], v[172:175]\n\t"
      "v_exp_f32 v90, v90\n\t"
      "v_mfma_f32_32x32x16_bf16 v[36:51], a[120:123], a[20:23], v[36:51]\n\t"
      "v_exp_f32 v91, v91\n\t"
      "v_cvt_pk_bf16_f32 v84, v84, v85\n\t"
      "v_cvt_pk_bf16_f32 v85, v86, v87\n\t"
      "v_cvt_pk_bf16_f32 v86, v88, v89\n\t"
      "v_cvt_pk_bf16_f32 v87, v90, v91\n\t"
      "v_mfma_f32_32x32x16_bf16 v[36:51], a[124:127], a[136:139], v[36:51]\n\t"
      "v_exp_f32 v52, v52\n\t"
      "v_exp_f32 v53, v53\n\t"
      "v_cvt_pk_bf16_f32 v88, v20, v21\n\t"
      "v_cvt_pk_bf16_f32 v148, v52, v53\n\t"
      "v_mfma_f32_32x32x16_bf16 v[4:19], v[228:231], v[84:87], v[4:19]\n\t"
      "v_cvt_pk_bf16_f32 v89, v22, v23\n\t"
      "v_cvt_pk_bf16_f32 v90, v24, v25\n\t"
      "v_cvt_pk_bf16_f32 v91, v26, v27\n\t"
      "v_cvt_pk_bf16_f32 v176, v28, v29\n\t"
      "v_cvt_pk_bf16_f32 v177, v30, v31\n\t"
      "v_cvt_pk_bf16_f32 v178, v32, v33\n\t"
      "v_mfma_f32_16x16x32_bf16 v[172:175], a[24:27], v[84:87], v[172:175]\n\t"
      "v_cvt_pk_bf16_f32 v179, v34, v35\n\t"
      "v_exp_f32 v132, v132\n\t"
      "v_mfma_f32_32x32x16_bf16 v[4:19], v[152:155], v[148:151], v[4:19]\n\t"
      "v_exp_f32 v133, v133\n\t"
      "v_exp_f32 v134, v134\n\t"
      "v_exp_f32 v135, v135\n\t"
      "v_mfma_f32_16x16x32_bf16 v[172:175], a[24:27], v[148:151], v[172:175]\n\t"
      "v_exp_f32 v136, v136\n\t"
      "v_mfma_f32_32x32x16_bf16 v[36:51], a[128:131], a[140:143], v[36:51]\n\t"
      "v_exp_f32 v137, v137\n\t"
      "v_exp_f32 v138, v138\n\t"
      "v_exp_f32 v139, v139\n\t"
      "v_mfma_f32_32x32x16_bf16 v[20:35], v[160:163], v[88:91], 0\n\t"
      "v_cvt_pk_bf16_f32 v132, v132, v133\n\t"
      "v_cvt_pk_bf16_f32 v133, v134, v135\n\t"
      "v_cvt_pk_bf16_f32 v134, v136, v137\n\t"
      "v_cvt_pk_bf16_f32 v135, v138, v139\n\t"
      "v_exp_f32 v140, v140\n\t"
      "v_mfma_f32_32x32x16_bf16 v[20:35], v[164:167], v[176:179], v[20:35]\n\t"
      "v_exp_f32 v141, v141\n\t"
      "v_exp_f32 v142, v142\n\t"
      "v_exp_f32 v143, v143\n\t"
      "v_mfma_f32_32x32x16_bf16 v[92:107], v[224:227], v[132:135], v[92:107]\n\t"
      "v_exp_f32 v144, v144\n\t"
      "v_exp_f32 v145, v145\n\t"
      "v_exp_f32 v146, v146\n\t"
      "v_mfma_f32_16x16x32_bf16 v[216:219], a[24:27], v[132:135], v[216:219]\n\t"
      "v_exp_f32 v147, v147\n\t"
      "v_mfma_f32_32x32x16_bf16 v[52:67], a[116:119], a[144:147], 0\n\t"
      "v_cvt_pk_bf16_f32 v140, v140, v141\n\t"
      "v_cvt_pk_bf16_f32 v141, v142, v143\n\t"
      "v_cvt_pk_bf16_f32 v142, v144, v145\n\t"
      "v_cvt_pk_bf16_f32 v143, v146, v147\n\t"
      "v_exp_f32 v68, v68\n\t"
      "v_mfma_f32_32x32x16_bf16 v[52:67], a[120:123], a[148:151], v[52:67]\n\t"
      "v_exp_f32 v69, v69\n\t"
      "v_rcp_f32 v2, v172\n\t"
      "v_cvt_pk_bf16_f32 v148, v68, v69\n\t"
      "v_subrev_u32 v3, 0x2b800000, v172\n\t"
      "v_mfma_f32_32x32x16_bf16 v[92:107], v[228:231], v[140:143], v[92:107]\n\t"
      "v_max_u32 v234, v234, v3\n\t"
      "v_fmac_f32 v36, v4, v2\n\t"
      "v_fmac_f32 v37, v5, v2\n\t"
      "v_cvt_pk_bf16_f32 v36, v36, v37\n\t"
      "v_pk_max_i16 v36, v36, 0\n\t"
      "v_fmac_f32 v38, v6, v2\n\t"
      "v_mfma_f32_16x16x32_bf16 v[216:219], a[24:27], v[140:143], v[216:219]\n\t"
      "v_fmac_f32 v39, v7, v2\n\t"
      "v_cvt_pk_bf16_f32 v37, v38, v39\n\t"
      "v_mfma_f32_32x32x16_bf16 v[92:107], v[152:155], v[148:151], v[92:107]\n\t"
      "v_pk_max_i16 v37, v37, 0\n\t"
      "v_fmac_f32 v40, v8, v2\n\t"
      "v_fmac_f32 v41, v9, v2\n\t"
      "v_cvt_pk_bf16_f32 v38, v40, v41\n\t"
      "v_pk_max_i16 v38, v38, 0\n\t"
      "v_fmac_f32 v42, v10, v2\n\t"
      "v_mfma_f32_16x16x32_bf16 v[216:219], a[24:27], v[148:151], v[216:219]\n\t"
      "v_fmac_f32 v43, v11, v2\n\t"
      "v_cvt_pk_bf16_f32 v39, v42, v43\n\t"
      "v_mfma_f32_32x32x16_bf16 v[52:67], a[124:127], a[152:155], v[52:67]\n\t"
      "v_pk_max_i16 v39, v39, 0\n\t"
      "v_fmac_f32 v44, v12, v2\n\t"
      "v_fmac_f32 v45, v13, v2\n\t"
      "v_cvt_pk_bf16_f32 v40, v44, v45\n\t"
      "v_pk_max_i16 v40, v40, 0\n\t"
      "v_fmac_f32 v46, v14, v2\n\t"
      "v_mfma_f32_32x32x16_bf16 v[52:67], a[128:131], a[156:159], v[52:67]\n\t"
      "v_fmac_f32 v47, v15, v2\n\t"
      "v_cvt_pk_bf16_f32 v41, v46, v47\n\t"
      "v_pk_max_i16 v41, v41, 0\n\t"
      "v_fmac_f32 v48, v16, v2\n\t"
      "v_fmac_f32 v49, v17, v2\n\t"
      "v_cvt_pk_bf16_f32 v42, v48, v49\n\t"
      "v_mfma_f32_32x32x16_bf16 v[68:83], v[192:195], v[88:91], 0\n\t"
      "v_pk_max_i16 v42, v42, 0\n\t"
      "v_fmac_f32 v50, v18, v2\n\t"
      "v_fmac_f32 v51, v19, v2\n\t"
      "v_cvt_pk_bf16_f32 v43, v50, v51\n\t"
      "v_pk_max_i16 v43, v43, 0\n\t"
      "ds_write_b128 v235, v[36:39]\n\t"
      "v_mfma_f32_32x32x16_bf16 v[68:83], v[196:199], v[176:179], v[68:83]\n\t"
      "ds_write_b128 v236, v[40:43]\n\t"
      "v_mfma_f32_32x32x16_bf16 v[4:19], a[36:39], a[104:107], 0\n\t"
      "v_exp_f32 v20, v20\n\t"
      "v_exp_f32 v21, v21\n\t"
      "ds_read_b128 a[0:3], v237\n\t"
      "v_cvt_pk_bf16_f32 v20, v20, v21\n\t"
      "v_mfma_f32_32x32x16_bf16 v[4:19], a[40:43], a[108:111], v[4:19]\n\t"
      "ds_read_b128 a[4:7], v237 offset:1024\n\t"
      "v_exp_f32 v22, v22\n\t"
      "v_exp_f32 v23, v23\n\t"
      "v_exp_f32 v24, v24\n\t"
      "v_mfma_f32_32x32x16_bf16 v[4:19], a[44:47], a[112:115], v[4:19]\n\t"
      "v_exp_f32 v25, v25\n\t"
      "v_exp_f32 v26, v26\n\t"
      "v_exp_f32 v27, v27\n\t"
      "v_mfma_f32_32x32x16_bf16 v[4:19], a[48:51], a[160:163], v[4:19]\n\t"
      "v_cvt_pk_bf16_f32 v21, v22, v23\n\t"
      "v_cvt_pk_bf16_f32 v22, v24, v25\n\t"
      "v_cvt_pk_bf16_f32 v23, v26, v27\n\t"
      "v_exp_f32 v28, v28\n\t"
      "v_subrev_u32 v2, 0x2b800000, v216\n\t"
      "v_mfma_f32_32x32x16_bf16 v[36:51], v[184:187], v[20:23], 0\n\t"
      "v_exp_f32 v29, v29\n\t"
      "v_exp_f32 v30, v30\n\t"
      "v_exp_f32 v31, v31\n\t"
      "v_mfma_f32_16x16x32_bf16 v[84:87], a[24:27], v[20:23], 0\n\t"
      "s_waitcnt lgkmcnt(1)\n\t"
      "global_store_dwordx4 v232, a[0:3], s[76:77] sc1 nt\n\t"
      "v_mfma_f32_32x32x16_bf16 v[108:123], v[200:203], v[88:91], 0\n\t"
      "s_waitcnt lgkmcnt(0)\n\t"
      "global_store_dwordx4 v232, a[4:7], s[76:77] offset:2048 sc1 nt\n\t"
      "v_exp_f32 v32, v32\n\t"
      "v_cvt_pk_bf16_f32 v28, v28, v29\n\t"
      "v_mfma_f32_32x32x16_bf16 v[108:123], v[204:207], v[176:179], v[108:123]\n\t"
      "v_exp_f32 v33, v33\n\t"
      "v_exp_f32 v34, v34\n\t"
      "v_exp_f32 v35, v35\n\t"
      "v_mfma_f32_16x16x32_bf16 v[132:135], v[124:127], v[88:91], 0\n\t"
      "v_cvt_pk_bf16_f32 v29, v30, v31\n\t"
      "v_cvt_pk_bf16_f32 v30, v32, v33\n\t"
      "v_mfma_f32_16x16x32_bf16 v[132:135], v[128:131], v[176:179], v[132:135]\n\t"
      "v_cvt_pk_bf16_f32 v31, v34, v35\n\t"
      "v_max_u32 v234, v234, v2\n\t"
      "v_mfma_f32_16x16x32_bf16 v[88:91], a[116:119], a[104:107], 0\n\t"
      "v_rcp_f32 v2, v216\n\t"
      "v_mfma_f32_32x32x16_bf16 v[36:51], v[188:191], v[28:31], v[36:51]\n\t"
      "v_fmac_f32 v52, v92, v2\n\t"
      "v_fmac_f32 v53, v93, v2\n\t"
      "v_cvt_pk_bf16_f32 v52, v52, v53\n\t"
      "v_pk_max_i16 v52, v52, 0\n\t"
      "v_fmac_f32 v54, v94, v2\n\t"
      "v_fmac_f32 v55, v95, v2\n\t"
      "v_mfma_f32_16x16x32_bf16 v[84:87], a[24:27], v[28:31], v[84:87]\n\t"
      "v_cvt_pk_bf16_f32 v53, v54, v55\n\t"
      "v_pk_max_i16 v53, v53, 0\n\t"
      "v_mfma_f32_16x16x32_bf16 v[88:91], a[120:123], a[108:111], v[88:91]\n\t"
      "v_fmac_f32 v56, v96, v2\n\t"
      "v_fmac_f32 v57, v97, v2\n\t"
      "v_mfma_f32_16x16x32_bf16 v[88:91], a[124:127], a[112:115], v[88:91]\n\t"
      "v_cvt_pk_bf16_f32 v54, v56, v57\n\t"
      "v_pk_max_i16 v54, v54, 0\n\t"
      "v_mfma_f32_16x16x32_bf16 v[88:91], a[128:131], a[160:163], v[88:91]\n\t"
      "v_fmac_f32 v58, v98, v2\n\t"
      "v_fmac_f32 v59, v99, v2\n\t"
      "v_mfma_f32_32x32x16_bf16 v[20:35], a[116:119], a[88:91], 0\n\t"
      "v_cvt_pk_bf16_f32 v55, v58, v59\n\t"
      "v_pk_max_i16 v55, v55, 0\n\t"
      "v_fmac_f32 v60, v100, v2\n\t"
      "v_fmac_f32 v61, v101, v2\n\t"
      "v_cvt_pk_bf16_f32 v56, v60, v61\n\t"
      "v_pk_max_i16 v56, v56, 0\n\t"
      "v_fmac_f32 v62, v102, v2\n\t"
      "v_fmac_f32 v63, v103, v2\n\t"
      "v_cvt_pk_bf16_f32 v57, v62, v63\n\t"
      "v_pk_max_i16 v57, v57, 0\n\t"
      "v_fmac_f32 v64, v104, v2\n\t"
      "v_fmac_f32 v65, v105, v2\n\t"
      "v_cvt_pk_bf16_f32 v58, v64, v65\n\t"
      "v_pk_max_i16 v58, v58, 0\n\t"
      "v_fmac_f32 v66, v106, v2\n\t"
      "v_fmac_f32 v67, v107, v2\n\t"
      "v_cvt_pk_bf16_f32 v59, v66, v67\n\t"
      "v_pk_max_i16 v59, v59, 0\n\t"
      "v_mfma_f32_32x32x16_bf16 v[20:35], a[120:123], a[92:95], v[20:35]\n\t"
      "ds_write_b128 v235, v[52:55]\n\t"
      "v_mfma_f32_32x32x16_bf16 v[20:35], a[124:127], a[96:99], v[20:35]\n\t"
      "ds_write_b128 v236, v[56:59]\n\t"
      "v_mfma_f32_32x32x16_bf16 v[20:35], a[128:131], a[100:103], v[20:35]\n\t"
      "v_cvt_pk_bf16_f32 v52, v4, v5\n\t"
      "v_cvt_pk_bf16_f32 v53, v6, v7\n\t"
      "v_cvt_pk_bf16_f32 v54, v8, v9\n\t"
      "v_cvt_pk_bf16_f32 v55, v10, v11\n\t"
      "ds_read_b128 a[0:3], v237\n\t"
      "v_cvt_pk_bf16_f32 v4, v12, v13\n\t"
      "v_mfma_f32_16x16x32_bf16 v[8:11], v[160:163], v[52:55], 0\n\t"
      "ds_read_b128 a[4:7], v237 offset:1024\n\t"
      "v_cvt_pk_bf16_f32 v5, v14, v15\n\t"
      "v_mfma_f32_16x16x32_bf16 v[12:15], v[192:195], v[52:55], 0\n\t"
      "v_cvt_pk_bf16_f32 v6, v16, v17\n\t"
      "v_cvt_pk_bf16_f32 v7, v18, v19\n\t"
      "v_mfma_f32_16x16x32_bf16 v[16:19], v[200:203], v[52:55], 0\n\t"
      "v_exp_f32 v68, v68\n\t"
      "v_mfma_f32_16x16x32_bf16 v[8:11], v[164:167], v[4:7], v[8:11]\n\t"
      "v_exp_f32 v69, v69\n\t"
      "v_mfma_f32_16x16x32_bf16 v[12:15], v[196:199], v[4:7], v[12:15]\n\t"
      "v_exp_f32 v70, v70\n\t"
      "v_mfma_f32_16x16x32_bf16 v[16:19], v[204:207], v[4:7], v[16:19]\n\t"
      "v_exp_f32 v71, v71\n\t"
      "v_mfma_f32_16x16x32_bf16 v[56:59], v[124:127], v[52:55], v[180:183]\n\t"
      "v_exp_f32 v72, v72\n\t"
      "v_mfma_f32_16x16x32_bf16 v[56:59], v[128:131], v[4:7], v[56:59]\n\t"
      "v_exp_f32 v73, v73\n\t"
      "v_exp_f32 v74, v74\n\t"
      "v_exp_f32 v75, v75\n\t"
      "s_waitcnt lgkmcnt(1)\n\t"
      "global_store_dwordx4 v232, a[0:3], s[78:79] sc1 nt\n\t"
      "s_waitcnt lgkmcnt(0)\n\t"
      "global_store_dwordx4 v232, a[4:7], s[78:79] offset:2048 sc1 nt\n\t"
      "v_cvt_pk_bf16_f32 v68, v68, v69\n\t"
      "v_cvt_pk_bf16_f32 v69, v70, v71\n\t"
      "v_cvt_pk_bf16_f32 v70, v72, v73\n\t"
      "v_cvt_pk_bf16_f32 v71, v74, v75\n\t"
      "v_exp_f32 v76, v76\n\t"
      "v_exp_f32 v77, v77\n\t"
      "v_mfma_f32_32x32x16_bf16 v[36:51], v[156:159], v[68:71], v[36:51]\n\t"
      "v_exp_f32 v78, v78\n\t"
      "v_exp_f32 v79, v79\n\t"
      "v_exp_f32 v80, v80\n\t"
      "v_mfma_f32_16x16x32_bf16 v[84:87], a[24:27], v[68:71], v[84:87]\n\t"
      "v_exp_f32 v81, v81\n\t"
      "v_exp_f32 v82, v82\n\t"
      "v_exp_f32 v83, v83\n\t"
      "v_cvt_pk_bf16_f32 v76, v76, v77\n\t"
      "v_cvt_pk_bf16_f32 v77, v78, v79\n\t"
      "v_cvt_pk_bf16_f32 v78, v80, v81\n\t"
      "v_cvt_pk_bf16_f32 v79, v82, v83\n\t"
      "v_exp_f32 v132, v132\n\t"
      "v_exp_f32 v133, v133\n\t"
      "v_mfma_f32_32x32x16_bf16 v[36:51], v[168:171], v[76:79], v[36:51]\n\t"
      "v_cvt_pk_bf16_f32 v148, v132, v133\n\t"
      "v_exp_f32 v108, v108\n\t"
      "v_exp_f32 v109, v109\n\t"
      "v_exp_f32 v110, v110\n\t"
      "v_mfma_f32_16x16x32_bf16 v[84:87], a[24:27], v[76:79], v[84:87]\n\t"
      "v_exp_f32 v111, v111\n\t"
      "v_mfma_f32_32x32x16_bf16 v[36:51], v[152:155], v[148:151], v[36:51]\n\t"
      "v_exp_f32 v112, v112\n\t"
      "v_exp_f32 v113, v113\n\t"
      "v_exp_f32 v114, v114\n\t"
      "v_mfma_f32_16x16x32_bf16 v[84:87], a[24:27], v[148:151], v[84:87]\n\t"
      "v_exp_f32 v115, v115\n\t"
      "v_cvt_pk_bf16_f32 v108, v108, v109\n\t"
      "v_cvt_pk_bf16_f32 v109, v110, v111\n\t"
      "v_cvt_pk_bf16_f32 v110, v112, v113\n\t"
      "v_cvt_pk_bf16_f32 v111, v114, v115\n\t"
      "v_exp_f32 v116, v116\n\t"
      "v_exp_f32 v117, v117\n\t"
      "v_mfma_f32_32x32x16_bf16 v[36:51], v[224:227], v[108:111], v[36:51]\n\t"
      "v_exp_f32 v118, v118\n\t"
      "v_exp_f32 v119, v119\n\t"
      "v_exp_f32 v120, v120\n\t"
      "v_mfma_f32_16x16x32_bf16 v[84:87], a[24:27], v[108:111], v[84:87]\n\t"
      "v_exp_f32 v121, v121\n\t"
      "v_exp_f32 v122, v122\n\t"
      "v_exp_f32 v123, v123\n\t"
      "v_cvt_pk_bf16_f32 v116, v116, v117\n\t"
      "v_cvt_pk_bf16_f32 v117, v118, v119\n\t"
      "v_cvt_pk_bf16_f32 v118, v120, v121\n\t"
      "v_cvt_pk_bf16_f32 v119, v122, v123\n\t"
      "v_exp_f32 v8, v8\n\t"
      "v_exp_f32 v9, v9\n\t"
      "v_mfma_f32_32x32x16_bf16 v[36:51], v[228:231], v[116:119], v[36:51]\n\t"
      "v_exp_f32 v10, v10\n\t"
      "v_exp_f32 v11, v11\n\t"
      "v_cvt_pk_bf16_f32 v8, v8, v9\n\t"
      "v_cvt_pk_bf16_f32 v9, v10, v11\n\t"
      "v_mfma_f32_16x16x32_bf16 v[84:87], a[24:27], v[116:119], v[84:87]\n\t"
      "ds_write_b64 v238, v[8:9] offset:0\n\t"
      "v_exp_f32 v12, v12\n\t"
      "v_exp_f32 v13, v13\n\t"
      "v_exp_f32 v14, v14\n\t"
      "v_exp_f32 v15, v15\n\t"
      "v_cvt_pk_bf16_f32 v12, v12, v13\n\t"
      "v_cvt_pk_bf16_f32 v13, v14, v15\n\t"
      "ds_write_b64 v238, v[12:13] offset:64\n\t"
      "v_exp_f32 v16, v16\n\t"
      "v_exp_f32 v17, v17\n\t"
      "v_exp_f32 v18, v18\n\t"
      "v_exp_f32 v19, v19\n\t"
      "v_cvt_pk_bf16_f32 v16, v16, v17\n\t"
      "v_cvt_pk_bf16_f32 v17, v18, v19\n\t"
      "ds_write_b64 v238, v[16:17] offset:128\n\t"
      "v_exp_f32 v56, v56\n\t"
      "v_exp_f32 v57, v57\n\t"
      "v_rcp_f32 v2, v84\n\t"
      "v_cvt_pk_bf16_f32 v56, v56, v57\n\t"
      "v_mov_b32 v57, 0\n\t"
      "ds_write_b64 v239, v[56:57] offset:192\n\t"
      "ds_read_b128 a[0:3], v1 offset:0\n\t"
      "ds_read_b128 a[4:7], v1 offset:32\n\t"
      "v_subrev_u32 v3, 0x2b800000, v84\n\t"
      "v_max_u32 v234, v234, v3\n\t"
      "v_fmac_f32 v20, v36, v2\n\t"
      "ds_read_b128 a[8:11], v1 offset:64\n\t"
      "v_fmac_f32 v21, v37, v2\n\t"
      "v_cvt_pk_bf16_f32 v20, v20, v21\n\t"
      "v_pk_max_i16 v20, v20, 0\n\t"
      "ds_read_b128 a[12:15], v1 offset:96\n\t"
      "v_fmac_f32 v22, v38, v2\n\t"
      "v_fmac_f32 v23, v39, v2\n\t"
      "v_cvt_pk_bf16_f32 v21, v22, v23\n\t"
      "ds_read_b128 a[16:19], v1 offset:128\n\t"
      "v_pk_max_i16 v21, v21, 0\n\t"
      "v_fmac_f32 v24, v40, v2\n\t"
      "v_fmac_f32 v25, v41, v2\n\t"
      "ds_read_b128 a[20:23], v1 offset:160\n\t"
      "v_cvt_pk_bf16_f32 v22, v24, v25\n\t"
      "v_pk_max_i16 v22, v22, 0\n\t"
      "v_fmac_f32 v26, v42, v2\n\t"
      "ds_read_b128 a[24:27], v1 offset:192\n\t"
      "v_fmac_f32 v27, v43, v2\n\t"
      "v_cvt_pk_bf16_f32 v23, v26, v27\n\t"
      "v_pk_max_i16 v23, v23, 0\n\t"
      "v_fmac_f32 v28, v44, v2\n\t"
      "v_fmac_f32 v29, v45, v2\n\t"
      "v_cvt_pk_bf16_f32 v24, v28, v29\n\t"
      "v_pk_max_i16 v24, v24, 0\n\t"
      "v_fmac_f32 v30, v46, v2\n\t"
      "v_fmac_f32 v31, v47, v2\n\t"
      "v_cvt_pk_bf16_f32 v25, v30, v31\n\t"
      "v_pk_max_i16 v25, v25, 0\n\t"
      "s_waitcnt lgkmcnt(6)\n\t"
      "v_mfma_f32_16x16x32_bf16 v[4:7], v[184:187], a[0:3], 0\n\t"
      "v_fmac_f32 v32, v48, v2\n\t"
      "v_fmac_f32 v33, v49, v2\n\t"
      "v_mfma_f32_16x16x32_bf16 v[8:11], a[132:135], a[0:3], 0\n\t"
      "v_cvt_pk_bf16_f32 v26, v32, v33\n\t"
      "s_waitcnt lgkmcnt(4)\n\t"
      "v_mfma_f32_16x16x32_bf16 v[4:7], v[188:191], a[4:7], v[4:7]\n\t"
      "v_pk_max_i16 v26, v26, 0\n\t"
      "v_fmac_f32 v34, v50, v2\n\t"
      "v_mfma_f32_16x16x32_bf16 v[8:11], a[132:135], a[4:7], v[8:11]\n\t"
      "v_fmac_f32 v35, v51, v2\n\t"
      "v_cvt_pk_bf16_f32 v27, v34, v35\n\t"
      "v_mfma_f32_16x16x32_bf16 v[4:7], v[156:159], a[8:11], v[4:7]\n\t"
      "v_pk_max_i16 v27, v27, 0\n\t"
      "ds_write_b128 v235, v[20:23]\n\t"
      "v_mfma_f32_16x16x32_bf16 v[8:11], a[132:135], a[8:11], v[8:11]\n\t"
      "ds_write_b128 v236, v[24:27]\n\t"
      "s_waitcnt lgkmcnt(2)\n\t"
      "v_mfma_f32_16x16x32_bf16 v[4:7], v[168:171], a[12:15], v[4:7]\n\t"
      "s_nop 1\n\t"
      "v_mfma_f32_16x16x32_bf16 v[8:11], a[132:135], a[12:15], v[8:11]\n\t"
      "ds_read_b128 a[0:3], v237\n\t"
      "v_mfma_f32_16x16x32_bf16 v[4:7], v[224:227], a[16:19], v[4:7]\n\t"
      "ds_read_b128 a[4:7], v237 offset:1024\n\t"
      "v_mfma_f32_16x16x32_bf16 v[8:11], a[132:135], a[16:19], v[8:11]\n\t"
      "s_nop 1\n\t"
      "v_mfma_f32_16x16x32_bf16 v[4:7], v[228:231], a[20:23], v[4:7]\n\t"
      "s_nop 1\n\t"
      "v_mfma_f32_16x16x32_bf16 v[8:11], a[132:135], a[20:23], v[8:11]\n\t"
      "s_nop 1\n\t"
      "v_mfma_f32_16x16x32_bf16 v[4:7], v[152:155], a[24:27], v[4:7]\n\t"
      "s_nop 1\n\t"
      "v_mfma_f32_16x16x32_bf16 v[8:11], a[132:135], a[24:27], v[8:11]\n\t"
      "s_nop 7\n\t"
      "v_rcp_f32 v1, v8\n\t"
      "s_mov_b64 exec, s[54:55]\n\tv_subrev_u32 v2, 0x2b800000, v8\n\tv_max_u32 v234, v234, v2\n\ts_mov_b64 exec, -1\n\t"
      "v_fma_f32 v4, v4, v1, v88\n\t"
      "s_waitcnt lgkmcnt(1)\n\t"
      "global_store_dwordx4 v232, a[0:3], s[80:81] sc1 nt\n\t"
      "s_waitcnt lgkmcnt(0)\n\t"
      "global_store_dwordx4 v232, a[4:7], s[80:81] offset:2048 sc1 nt\n\t"
      "v_fma_f32 v5, v5, v1, v89\n\t"
      "v_cvt_pk_bf16_f32 v4, v4, v5\n\t"
      "v_pk_max_i16 v4, v4, 0\n\t"
      "v_fma_f32 v6, v6, v1, v90\n\t"
      "v_fma_f32 v7, v7, v1, v91\n\t"
      "v_cvt_pk_bf16_f32 v5, v6, v7\n\t"
      "v_pk_max_i16 v5, v5, 0\n\t"
      "s_mov_b64 exec, s[54:55]\n\tglobal_store_dwordx2 v240, v[4:5], s[76:77] sc1 nt\n\ts_mov_b64 exec, -1\n\t"
      "Lend_%=:\n\t"
      "v_cmp_lt_u32 vcc, 0x28000000, v233\n\t"
      "s_nop 1\n\t"
      "s_mov_b64 %[bad0], vcc\n\t"
      "v_cmp_lt_u32 vcc, 0x28000000, v234\n\t"
      "s_nop 1\n\t"
      "s_mov_b64 %[bad1], vcc\n\t"
      : [bad0] "=&s"(bad0), [bad1] "=&s"(bad1)
      : [tid] "v"(threadIdx.x), [bid] "s"(blockIdx.x), [ka] "s"(__builtin_amdgcn_kernarg_segment_ptr())
      : "v1", "v2", "v3", "v4", "v5", "v6", "v7", "v8", "v9", "v10", "v11", "v12", "v13", "v14", "v15", "v16", "v17", "v18", "v19", "v20", "v21", "v22", "v23", "v24", "v25", "v26", "v27", "v28", "v29", "v30", "v31", "v32", "v33", "v34", "v35", "v36", "v37", "v38", "v39", "v40", "v41", "v42", "v43", "v44", "v45", "v46", "v47", "v48", "v49", "v50", "v51", "v52", "v53", "v54", "v55", "v56", "v57", "v58", "v59", "v60", "v61", "v62", "v63", "v64", "v65", "v66", "v67", "v68", "v69", "v70", "v71", "v72", "v73", "v74", "v75", "v76", "v77", "v78", "v79", "v80", "v81", "v82", "v83", "v84", "v85", "v86", "v87", "v88", "v89", "v90", "v91", "v92", "v93", "v94", "v95", "v96", "v97", "v98", "v99", "v100", "v101", "v102", "v103", "v104", "v105", "v106", "v107", "v108", "v109", "v110", "v111", "v112", "v113", "v114", "v115", "v116", "v117", "v118", "v119", "v120", "v121", "v122", "v123", "v124", "v125", "v126", "v127", "v128", "v129", "v130", "v131", "v132", "v133", "v134", "v135", "v136", "v137", "v138", "v139", "v140", "v141", "v142", "v143", "v144", "v145", "v146", "v147", "v148", "v149", "v150", "v151", "v152", "v153", "v154", "v155", "v156", "v157", "v158", "v159", "v160", "v161", "v162", "v163", "v164", "v165", "v166", "v167", "v168", "v169", "v170", "v171", "v172", "v173", "v174", "v175", "v176", "v177", "v178", "v179", "v180", "v181", "v182", "v183", "v184", "v185", "v186", "v187", "v188", "v189", "v190", "v191", "v192", "v193", "v194", "v195", "v196", "v197", "v198", "v199", "v200", "v201", "v202", "v203", "v204", "v205", "v206", "v207", "v208", "v209", "v210", "v211", "v212", "v213", "v214", "v215", "v216", "v217", "v218", "v219", "v220", "v221", "v222", "v223", "v224", "v225", "v226", "v227", "v228", "v229", "v230", "v231", "v232", "v233", "v234", "v235", "v236", "v237", "v238", "v239", "v240", "v241", "v242", "v243", "v244", "v245", "v246", "v247", "v248", "v249", "v250", "v251", "v252", "v253", "v254", "v255", "a0", "a1", "a2", "a3", "a4", "a5", "a6", "a7", "a8", "a9", "a10", "a11", "a12", "a13", "a14", "a15", "a16", "a17", "a18", "a19", "a20", "a21", "a22", "a23", "a24", "a25", "a26", "a27", "a28", "a29", "a30", "a31", "a32", "a33", "a34", "a35", "a36", "a37", "a38", "a39", "a40", "a41", "a42", "a43", "a44", "a45", "a46", "a47", "a48", "a49", "a50", "a51", "a52", "a53", "a54", "a55", "a56", "a57", "a58", "a59", "a60", "a61", "a62", "a63", "a64", "a65", "a66", "a67", "a68", "a69", "a70", "a71", "a72", "a73", "a74", "a75", "a76", "a77", "a78", "a79", "a80", "a81", "a82", "a83", "a84", "a85", "a86", "a87", "a88", "a89", "a90", "a91", "a92", "a93", "a94", "a95", "a96", "a97", "a98", "a99", "a100", "a101", "a102", "a103", "a104", "a105", "a106", "a107", "a108", "a109", "a110", "a111", "a112", "a113", "a114", "a115", "a116", "a117", "a118", "a119", "a120", "a121", "a122", "a123", "a124", "a125", "a126", "a127", "a128", "a129", "a130", "a131", "a132", "a133", "a134", "a135", "a136", "a137", "a138", "a139", "a140", "a141", "a142", "a143", "a144", "a145", "a146", "a147", "a148", "a149", "a150", "a151", "a152", "a153", "a154", "a155", "a156", "a157", "a158", "a159", "a160", "a161", "a162", "a163", "a164", "a165", "a166", "a167", "a168", "a169", "a170", "a171", "a172", "a173", "a174", "a175", "a176", "a177", "a178", "a179", "a180", "a181", "a182", "a183", "a184", "a185", "a186", "a187", "a188", "a189", "a190", "a191", "a192", "a193", "a194", "a195", "a196", "a197", "a198", "a199", "a200", "a201", "a202", "a203", "a204", "a205", "a206", "a207", "a208", "a209", "a210", "a211", "a212", "a213", "a214", "a215", "a216", "a217", "a218", "a219", "a220", "a221", "a222", "a223", "a224", "a225", "a226", "a227", "a228", "a229", "a230", "a231", "a232", "a233", "a234", "a235", "a236", "a237", "a238", "a239", "a240", "a241", "a242", "a243", "a244", "a245", "a246", "a247", "a248", "a249", "a250", "a251", "a252", "a253", "a254", "a255", "s36", "s37", "s38", "s39", "s40", "s41", "s42", "s43", "s44", "s45", "s46", "s47", "s48", "s49", "s50", "s51", "s52", "s53", "s54", "s55", "s56", "s57", "s58", "s59", "s60", "s61", "s62", "s63", "s64", "s65", "s66", "s67", "s68", "s69", "s70", "s71", "s72", "s73", "s74", "s75", "s76", "s77", "s78", "s79", "s80", "s81", "s82", "s83", "s84", "s85", "s86", "s87", "s88", "vcc", "scc", "memory");
  const int w = __builtin_amdgcn_readfirstlane(threadIdx.x >> 6);
  if (__builtin_expect((bad0 | bad1) != 0ull, 0)) {
    asm volatile("s_waitcnt vmcnt(0)" ::: "memory");
#pragma unroll 1
    for (int j = 0; j < 2; ++j) {
      if ((j ? bad1 : bad0) == 0ull) continue;
      const int lb = (w >> 1) + 2 * j;
      safe_path((lds_cptr)lds, w, lb, threadIdx.x & 63, out + (blockIdx.x * 4 + lb) * 12800u + (w & 1) * 64);
    }
  }
}

extern "C" void kernel_launch(void* const* d_in, const int* in_sizes, int n_in, void* d_out, int out_size, void* d_ws, size_t ws_size,
                              hipStream_t stream) {
  (void)in_sizes; (void)n_in; (void)out_size; (void)d_ws; (void)ws_size;
  (void)hipFuncSetAttribute((const void*)autoint_kernel, hipFuncAttributeMaxDynamicSharedMemorySize, LDS_BYTES);
  autoint_kernel<<<256, 256, LDS_BYTES, stream>>>((const char*)d_in[0], (const char*)d_in[1], (const char*)d_in[2], (const char*)d_in[3],
                                                   (const char*)d_in[4], (char*)d_out);
}
```
